```python
import torch

def _softmax(x, axis=-1):
    return torch.softmax(x, dim=axis)
def _gelu(x):
    return torch.nn.functional.gelu(x, approximate="tanh")
def _silu(x):
    return torch.nn.functional.silu(x)
def _sigmoid(x):
    return torch.sigmoid(x)
def _layernorm(x, axis=-1, eps=1e-5):
    m = x.mean(dim=axis, keepdim=True)
    v = x.var(dim=axis, keepdim=True, unbiased=False)
    return (x - m) / torch.sqrt(v + eps)
def _scatter_add(tgt, idx, src, axis=0):
    return tgt.clone().index_add_(axis, idx, src)
def _segment_sum(src, seg, n):
    out = torch.zeros((n,) + src.shape[1:], dtype=src.dtype)
    return out.index_add_(0, seg, src)
def _pad(x, pw):
    flat = tuple(v for pair in reversed(pw) for v in pair)
    return torch.nn.functional.pad(x, flat)

kernel_name = 'interleave_layers__sliding_local_dilated__geglu_ffn__prompt'

def setup_inputs():

    _w = (lambda a: (lambda t: t.to(torch.bfloat16) if t.is_floating_point() else t)(torch.as_tensor(a)))
    def _A_setup():
        B, S, D = 1, 2048, 512
        return {
            "x": _w((lambda *s: torch.randn(*s, dtype=torch.bfloat16) * 0.05)(B, S, D)),
            "wqkv": _w((lambda *s: torch.randn(*s, dtype=torch.bfloat16) * 0.05)(D, 3 * D)),
            "wo": _w((lambda *s: torch.randn(*s, dtype=torch.bfloat16) * 0.05)(D, D)),
        }
    _A_in = _A_setup()

    def _B_setup():
        B, S, D, F = 1, 2048, 512, 1376
        return {
            "x": _w((lambda *s: torch.randn(*s, dtype=torch.bfloat16) * 0.05)(B, S, D)),
            "w_gate": _w((lambda *s: torch.randn(*s, dtype=torch.bfloat16) * 0.05)(D, F)),
            "w_up": _w((lambda *s: torch.randn(*s, dtype=torch.bfloat16) * 0.05)(D, F)),
            "w_down": _w((lambda *s: torch.randn(*s, dtype=torch.bfloat16) * 0.05)(F, D)),
        }
    _B_in = _B_setup()

    return {
        "A_x": _A_in["x"],
        "A_wqkv": _A_in["wqkv"],
        "A_wo": _A_in["wo"],
        "B_x": _B_in["x"],
        "B_w_gate": _B_in["w_gate"],
        "B_w_up": _B_in["w_up"],
        "B_w_down": _B_in["w_down"],
    }

def reference(A_x, A_wqkv, A_wo, B_x, B_w_gate, B_w_up, B_w_down):
    def _A_ref(x, wqkv, wo):
        B, S, D = x.shape
        H = 32; Dh = D // H
        W, dil = 8, 2
        qkv = (lambda x, *d: x.permute(*d))((x @ wqkv).reshape(B, S, 3, H, Dh), 2, 0, 3, 1, 4)
        q, k, v = qkv[0], qkv[1], qkv[2]
        scale = Dh ** -0.5
        pos = torch.arange(S)
        off = torch.arange(W) * dil
        idx = pos[:, None] - off[None, :]
        mask = idx >= 0
        idx_c = torch.where(mask, idx, 0).reshape(-1)
        kg = k[:, :, idx_c, :].reshape(B, H, S, W, Dh)
        vg = v[:, :, idx_c, :].reshape(B, H, S, W, Dh)
        logits = torch.einsum("bhsd,bhswd->bhsw", q, kg) * scale
        logits = torch.where(mask[None, None, :, :], logits, -1e9)
        attn = _softmax(logits, axis=-1)
        o = torch.einsum("bhsw,bhswd->bhsd", attn, vg)
        return (lambda x, *d: x.permute(*d))(o, 0, 2, 1, 3).reshape(B, S, D) @ wo

    def _B_ref(x, w_gate, w_up, w_down):
        return (_gelu(x @ w_gate) * (x @ w_up)) @ w_down

    x = A_x
    x = x + _A_ref(_layernorm(x, axis=-1), A_wqkv, A_wo)
    x = x + _B_ref(_layernorm(x, axis=-1), B_w_gate, B_w_up, B_w_down)
    x = x + _A_ref(_layernorm(x, axis=-1), A_wqkv, A_wo)
    x = x + _B_ref(_layernorm(x, axis=-1), B_w_gate, B_w_up, B_w_down)
    return x
```

```cpp
#include <hip/hip_runtime.h>
#include <hip/hip_bf16.h>
#include <stdint.h>

#define GAS __attribute__((address_space(1)))
#define LAS __attribute__((address_space(3)))
#define DI __device__ __forceinline__
typedef unsigned v4u __attribute__((ext_vector_type(4)));
typedef unsigned v2u __attribute__((ext_vector_type(2)));
typedef float f32x2 __attribute__((ext_vector_type(2)));
typedef float f32x4 __attribute__((ext_vector_type(4)));
typedef float f32x16 __attribute__((ext_vector_type(16)));
typedef __bf16 bf16x8 __attribute__((ext_vector_type(8)));
typedef __bf16 bf16x2 __attribute__((ext_vector_type(2)));
typedef short s16x4 __attribute__((ext_vector_type(4)));
typedef GAS unsigned gu32;
#define RLX_AGENT __ATOMIC_RELAXED, __HIP_MEMORY_SCOPE_AGENT

#define STAMPS 0

#define SYPRIO 3
#define PDV 7
#define NBV 8
#define STSLEEP 4
#define D_S4 4
#define PACE 0
#define LDPRIO 0
#define PSLEEP 0
#define SSLEEP 0
#define EXP 0
#define A_O 2
#define A_S1 30
#define A_S3 36
#define P1RATE 76
#define TSV 52
#define D_O 0
#define A_X1 22
#define A_X2 76
namespace {
constexpr int S_ = 2048, D_ = 512, F_ = 1376;
constexpr int NWG = 256;
constexpr unsigned KB = 1024;
constexpr unsigned CTRL_OFF = 0;
constexpr unsigned CTRL_BYTES = 256 * 16 * 128;
constexpr unsigned TMO_OFF = CTRL_OFF + CTRL_BYTES;
constexpr unsigned CP_OFF = TMO_OFF + 4096;
constexpr unsigned W1_OFF = 1024 * KB;
constexpr unsigned W2_OFF = W1_OFF + 16 * 96 * KB;
constexpr unsigned W3_OFF = W2_OFF + 8 * 64 * KB;
constexpr unsigned W4_OFF = W3_OFF + 16 * 192 * KB;
constexpr unsigned XA_OFF = W4_OFF + 8 * 172 * KB;
constexpr unsigned X_SZ = 2048 * KB;
constexpr unsigned STA_OFF = XA_OFF + 2 * X_SZ;
constexpr unsigned ST_SZ = 64 * 16 * 32 * 8;
constexpr unsigned O32_OFF = STA_OFF + 2 * ST_SZ;
constexpr unsigned X32_OFF = O32_OFF + 2 * X_SZ;
constexpr unsigned STB_OFF = X32_OFF + 2 * X_SZ;
constexpr unsigned H32_OFF = STB_OFF + 2 * ST_SZ;
constexpr unsigned H32_SZ = 64 * 86 * KB;
constexpr unsigned STAMP_OFF = H32_OFF + 2 * H32_SZ;
constexpr unsigned WS_END = STAMP_OFF + 256 * 8 * 64 * 8;
constexpr int L_SLOT = 0;
constexpr int L_EXC = 128 * 1024;
constexpr int L_KV = L_EXC + 16 * 1024;
constexpr int L_C1 = L_KV + 5 * 2048;
constexpr int L_C3 = L_C1 + 96 * 4;
constexpr int L_ST = L_C3 + 192 * 4;
constexpr int L_FLG = L_ST + 2048;
constexpr int L_END = L_FLG + 32;
static_assert(L_END <= 160 * 1024, "LDS");
constexpr unsigned SPIN_CAP = 1u << 17, RETRY_CAP = 1u << 13;

DI v4u ldg16(__amdgpu_buffer_rsrc_t rs, unsigned voff, unsigned soff) { return __builtin_amdgcn_raw_buffer_load_b128(rs, voff, soff, 16); }
DI v2u ldg8(__amdgpu_buffer_rsrc_t rs, unsigned voff, unsigned soff) { return __builtin_amdgcn_raw_buffer_load_b64(rs, voff, soff, 16); }
DI void stg16(v4u v, __amdgpu_buffer_rsrc_t rs, unsigned voff, unsigned soff) {
  __builtin_amdgcn_raw_buffer_store_b128(v, rs, voff, soff, 16);
  asm volatile("s_nop 1" ::"v"(v));
}
DI void stg8(v2u v, __amdgpu_buffer_rsrc_t rs, unsigned voff, unsigned soff) { __builtin_amdgcn_raw_buffer_store_b64(v, rs, voff, soff, 16); }
DI f32x4 mfma16(v4u a, v4u b, f32x4 c) { return __builtin_amdgcn_mfma_f32_16x16x32_bf16(__builtin_bit_cast(bf16x8, a), __builtin_bit_cast(bf16x8, b), c, 0, 0, 0); }
DI f32x4 mfma16k16(v2u a, v2u b, f32x4 c) { return __builtin_amdgcn_mfma_f32_16x16x16bf16_1k(__builtin_bit_cast(s16x4, a), __builtin_bit_cast(s16x4, b), c, 0, 0, 0); }
DI f32x16 mfma32(v4u a, v4u b, f32x16 c) { return __builtin_amdgcn_mfma_f32_32x32x16_bf16(__builtin_bit_cast(bf16x8, a), __builtin_bit_cast(bf16x8, b), c, 0, 0, 0); }
template <bool FIRST> DI void mma16(f32x4& c, v4u a, v4u b) {
  if constexpr (FIRST) asm volatile("v_mfma_f32_16x16x32_bf16 %0, %1, %2, 0" : "=&v"(c) : "v"(a), "v"(b));
  else asm volatile("v_mfma_f32_16x16x32_bf16 %0, %1, %2, %0" : "+v"(c) : "v"(a), "v"(b));
}
template <bool FIRST> DI void mma32(f32x16& c, v4u a, v4u b) {
  if constexpr (FIRST) asm volatile("v_mfma_f32_32x32x16_bf16 %0, %1, %2, 0" : "=&v"(c) : "v"(a), "v"(b));
  else asm volatile("v_mfma_f32_32x32x16_bf16 %0, %1, %2, %0" : "+v"(c) : "v"(a), "v"(b));
}
DI void mma32c(f32x16& d, v4u a, v4u b, const f32x16& c) { asm volatile("v_mfma_f32_32x32x16_bf16 %0, %1, %2, %3" : "=&v"(d) : "v"(a), "v"(b), "v"(c)); }
#define MFMA_DONE() asm volatile("s_nop 11" ::: "memory")
DI unsigned pk(float lo, float hi) { f32x2 v = {lo, hi}; return __builtin_bit_cast(unsigned, __builtin_convertvector(v, bf16x2)); }
DI float blo(unsigned d) { return __builtin_bit_cast(float, d << 16); }
DI float bhi(unsigned d) { return __builtin_bit_cast(float, d & 0xffff0000u); }
DI float u2f(unsigned u) { return __builtin_bit_cast(float, u); }
DI unsigned f2u(float f) { return __builtin_bit_cast(unsigned, f); }
DI float xsum32(float x) { auto r = __builtin_amdgcn_permlane32_swap(f2u(x), f2u(x), false, false); float d; asm("v_add_f32 %0, %1, %2" : "=v"(d) : "v"(u2f(r[0])), "v"(u2f(r[1]))); return d; }
DI float xsum16(float x) { auto r = __builtin_amdgcn_permlane16_swap(f2u(x), f2u(x), false, false); float d; asm("v_add_f32 %0, %1, %2" : "=v"(d) : "v"(u2f(r[0])), "v"(u2f(r[1]))); return d; }
DI float vmax(float a, float b) { float d; asm("v_max_f32 %0, %1, %2" : "=v"(d) : "v"(a), "v"(b)); return d; }
DI float sadd(float a, float b) { float d; asm("v_add_f32 %0, %1, %2" : "=v"(d) : "v"(a), "v"(b)); return d; }
DI float smul(float a, float b) { float d; asm("v_mul_f32 %0, %1, %2" : "=v"(d) : "v"(a), "v"(b)); return d; }
DI float sfma(float a, float b, float c) { float d; asm("v_fma_f32 %0, %1, %2, %3" : "=v"(d) : "v"(a), "v"(b), "v"(c)); return d; }
DI float vmax3(float a, float b, float c) { float d; asm("v_max3_f32 %0, %1, %2, %3" : "=v"(d) : "v"(a), "v"(b), "v"(c)); return d; }
DI float pick(bool p, float a, float b) { asm("" : "+v"(a)); return p ? b : a; }
DI float xmax32(float x) { auto r = __builtin_amdgcn_permlane32_swap(f2u(x), f2u(x), false, false); return vmax(u2f(r[0]), u2f(r[1])); }
DI float xmax16(float x) { auto r = __builtin_amdgcn_permlane16_swap(f2u(x), f2u(x), false, false); return vmax(u2f(r[0]), u2f(r[1])); }
DI float dot2(unsigned a, unsigned b, float c) { return __builtin_amdgcn_fdot2_f32_bf16(__builtin_bit_cast(bf16x2, a), __builtin_bit_cast(bf16x2, b), c, false); }
#define PIN(x) asm volatile("" : "+v"(x))
#define BAR() asm volatile("s_waitcnt lgkmcnt(0)\n\ts_barrier" ::: "memory")
#define DRAIN() asm volatile("s_waitcnt vmcnt(0)" ::: "memory")
#define WAITL(n) __builtin_amdgcn_s_waitcnt(0xC07F | ((n) << 8))
#define WAITV(n) asm volatile("s_waitcnt vmcnt(" #n ")" ::: "memory")

DI int unit_start(int j) { return j < 6 ? 6 * j : 36 + 5 * (j - 6); }
DI int hpos(int j, int lu) {
  if (lu < 4) return (lu & 1) * 43 + 16 * (lu >> 1) + j;
  if (lu == 4) return j < 11 ? 32 + j : 43 + 32 + (j - 11);
  return 43 + 37 + j;
}
DI int hunit(int kh, int s) {
  if (s < 32) return unit_start(s & 15) + 2 * (s >> 4) + kh;
  if (kh == 0) return unit_start(s - 32) + 4;
  return s < 37 ? unit_start(s - 32 + 11) + 4 : unit_start(s - 37) + 5;
}

DI f32x2 block_stats(const unsigned (&p)[8]) {
  float s0 = 0.f, s1 = 0.f, q0 = 0.f, q1 = 0.f;
#pragma unroll
  for (int i = 0; i < 8; i += 2) { s0 = dot2(p[i], 0x3f803f80u, s0); q0 = dot2(p[i], p[i], q0); s1 = dot2(p[i + 1], 0x3f803f80u, s1); q1 = dot2(p[i + 1], p[i + 1], q1); }
  return f32x2{xsum32(s0 + s1), xsum32(q0 + q1)};
}
DI float ln_var(float s, float q, float& mean) {
  mean = s * (1.f / 512.f);
  PIN(mean);
  return fmaxf(fmaf(-mean, mean, q * (1.f / 512.f)), 0.f) + 1e-5f;
}

constexpr unsigned FILL = 0xffffffffu;
DI unsigned nf(unsigned d) { return min(d, 0xfffffffeu); }
DI unsigned chk(unsigned m, v4u a) { return max(max(max(max(m, a.x), a.y), a.z), a.w); }

DI v4u gather8(const unsigned short* src, int stride, int k0, bool perm, int h) {
  unsigned v[8];
#pragma unroll
  for (int e = 0; e < 8; ++e) v[e] = src[(size_t)(k0 + (perm ? 8 * (e >> 2) + 4 * h + (e & 3) : 8 * h + e)) * stride];
  return v4u{v[0] | (v[1] << 16), v[2] | (v[3] << 16), v[4] | (v[5] << 16), v[6] | (v[7] << 16)};
}
__global__ void __launch_bounds__(512) k_prep(const unsigned short* __restrict__ x, const unsigned short* __restrict__ wqkv, const unsigned short* __restrict__ wo,
                                              const unsigned short* __restrict__ wg, const unsigned short* __restrict__ wu, const unsigned short* __restrict__ wd,
                                              unsigned char* __restrict__ ws) {
  const int lane = threadIdx.x & 63, wv = __builtin_amdgcn_readfirstlane(threadIdx.x >> 6), b = blockIdx.x;
  const int g = b >> 4, j = b & 15, n = lane & 31, h = lane >> 5;
  auto tile = [&](int t) {
    const bool valid = t < 8 || j < 6;
    const unsigned short* src = t < 3 ? wqkv + t * 512 + 32 * j + n : (n < 16 ? wg : wu) + (unit_start(j) + (valid ? t - 3 : 0)) * 16 + (n & 15);
    const int stride = t < 3 ? 1536 : F_;
    const v4u f0 = gather8(src, stride, 32 * g, true, h), f1 = gather8(src, stride, 32 * g + 16, true, h);
    const int lu = t - 3;
    const unsigned d0 = t < 3 ? W1_OFF + (j * 96 + 2 * g * 3 + t) * KB : W3_OFF + (j * 192 + (lu < 2 ? 4 * g + lu : lu * 32 + 2 * g)) * KB;
    const unsigned d1 = d0 + (t < 3 ? 3 : lu < 2 ? 2 : 1) * KB;
    *(v4u*)(ws + d0 + lane * 16) = f0;
    *(v4u*)(ws + d1 + lane * 16) = f1;
    float sm = 0.f;
#pragma unroll
    for (int i = 0; i < 4; ++i) { sm = dot2(f0[i], 0x3f803f80u, sm); sm = dot2(f1[i], 0x3f803f80u, sm); }
    sm = xsum32(sm);
    if (h == 0) ((float*)(ws + CP_OFF))[(j * 16 + g) * 288 + t * 32 + n] = sm;
  };
  tile(wv);
  if (wv == 0) tile(8);
  else {
    for (int f = b * 7 + wv - 1; f < 1888; f += 1792) {
      if (f < 512) *(v4u*)(ws + W2_OFF + f * KB + lane * 16) = gather8(wo + 64 * (f >> 6) + 32 * (f & 1) + n, 512, 16 * ((f & 63) >> 1), false, h);
      else {
        const int q = f - 512, cb = q / 172, r = q % 172;
        *(v4u*)(ws + W4_OFF + q * KB + lane * 16) = gather8(wd + 64 * cb + 32 * (r & 1) + n, 512, 16 * hunit((r >> 1) & 1, r >> 2), true, h);
      }
    }
  }
  if (wv >= 4) {
    const int f = b * 4 + wv - 4, t32 = f >> 4, cbk = f & 15;
    unsigned p[8];
#pragma unroll
    for (int q = 0; q < 4; ++q) { v2u v = *(const v2u*)(x + (size_t)(32 * t32 + n) * D_ + 32 * cbk + 8 * q + 4 * h); p[2 * q] = nf(v.x); p[2 * q + 1] = nf(v.y); }
    const f32x2 st = block_stats(p);
#pragma unroll
    for (int c = 0; c < 2; ++c) *(v4u*)(ws + XA_OFF + ((t32 * 32 + 2 * cbk + c) * 64 + lane) * 16) = v4u{p[4 * c], p[4 * c + 1], p[4 * c + 2], p[4 * c + 3]};
    if (h == 0) *(v2u*)(ws + STA_OFF + ((t32 * 16 + cbk) * 32 + n) * 8) = v2u{nf(f2u(st.x)), nf(f2u(st.y))};
  }
  const v4u ff = {~0u, ~0u, ~0u, ~0u};
  for (unsigned o = (b * 512 + threadIdx.x) * 16; o < H32_OFF - XA_OFF - X_SZ; o += 256 * 8192) {
    const unsigned a = XA_OFF + X_SZ + o;
    if (a < STA_OFF || a >= STA_OFF + ST_SZ) *(v4u*)(ws + a) = ff;
  }
  if (threadIdx.x < 128) ((v4u*)(ws + CTRL_OFF + b * 2048))[threadIdx.x] = v4u{0, 0, 0, 0};
  if (b == 0 && threadIdx.x == 128) *(v4u*)(ws + TMO_OFF) = v4u{0, 0, 0, 0};
}

enum { H_X = 0, H_O = 1, H_X1 = 2, H_H0 = 3, H_H1 = 4, H_H2 = 5 };
enum { P_V = 1, P_O = 2, P_X1 = 3, P_S3 = 4, P_H0 = 5, P_H1 = 6, P_H2 = 7, P_X2 = 8, P_EX = 9, Y_X = 1, Y_ST = 2, Y_O = 3, Y_X1 = 4, Y_H0 = 5, Y_H1 = 6, Y_H2 = 7, Z_W1 = 1, Z_W1B = 2, Z_W2 = 3, Z_W3 = 4 };
#define STAMP(idx) do {} while (0)
#define STAMP_RT(idx) do {} while (0)
#define STAMP_OUT() do {} while (0)

DI void dma_groups(__amdgpu_buffer_rsrc_t rs, unsigned src, LAS unsigned char* dst, int n, unsigned l16) {
#pragma unroll 1
  for (int i = 0; i < n; ++i) {
    __builtin_amdgcn_raw_ptr_buffer_load_lds(rs, (LAS void*)dst, 16, l16, src, 0, 0);
    __builtin_amdgcn_raw_ptr_buffer_load_lds(rs, (LAS void*)dst, 16, l16, src, 1024, 0);
    __builtin_amdgcn_raw_ptr_buffer_load_lds(rs, (LAS void*)dst, 16, l16, src, 2048, 0);
    __builtin_amdgcn_raw_ptr_buffer_load_lds(rs, (LAS void*)dst, 16, l16, src, 3072, 0);
    dst += 12288; src += 12288;
  }
}
template <int N> DI void dma_seg(__amdgpu_buffer_rsrc_t rs, unsigned src, LAS unsigned char* dst, int li, unsigned l16) { dma_groups(rs, src, dst, (N / 4 + 2 - li) / 3, l16); }
template <int I> struct IC { static constexpr int v = I; };
template <int B, int E, class Fn> DI void sfor(Fn&& f) { if constexpr (B < E) { f(IC<B>{}); sfor<B + 1, E>(f); } }
#define SB() __builtin_amdgcn_sched_barrier(0)
template <int B, int E, int NF, int PD, int GW, int SEG1, int SEG2, class FA, class FM, class FE> DI void wstream(v4u (&w)[NF], LAS unsigned char* L, FA&& addr, FM&& mma, FE&& extra) {
  sfor<B, E>([&](auto I) {
    constexpr int i = I.v, n = i + PD;
    constexpr bool hold = (SEG1 >= 0 && (n == SEG1 || n == SEG1 + 1)) || (SEG2 >= 0 && (n == SEG2 || n == SEG2 + 1));
    constexpr bool rel = (SEG1 >= 0 && n == SEG1 + 2) || (SEG2 >= 0 && n == SEG2 + 2);
    if constexpr (i % GW == 0 && n <= NF) WAITL(PD - GW);
    mma(I, w[i]);
    SB();
    if constexpr (rel) {
      BAR();
      w[n - 2] = *(LAS v4u*)(L + addr(IC<n - 2>{}));
      w[n - 1] = *(LAS v4u*)(L + addr(IC<n - 1>{}));
    }
    if constexpr (n < NF && !hold) w[n] = *(LAS v4u*)(L + addr(IC<n>{}));
    extra(I);
    SB();
  });
}
template <int PD, int NF, class FA> DI void wfirst(v4u (&w)[NF], LAS unsigned char* L, FA&& addr) {
  sfor<0, PD>([&](auto I) { w[I.v] = *(LAS v4u*)(L + addr(I)); });
  SB();
}
template <int B, int E, class FR> DI void paced(FR&& req) { sfor<B, E>([&](auto I) { if constexpr (I.v > B) asm volatile("s_nop %0" :: "i"(PACE)); req(I); SB(); }); }
constexpr int P1_N = 36;
constexpr int p1_items(int slots) { int n = 0; for (int i = 0; i < slots; ++i) n += ((i & 13) == 0) ? 2 : 1; return n; }
constexpr int p1_slot(int item) { int i = 0; while (p1_items(i + 1) <= item) ++i; return i; }
constexpr int p1_sl(int s) { return s < 63 ? s / 3 : 21 + (s - 63) % 11; }
constexpr int p1_mt(int s) { return s < 63 ? s % 3 : ((s - 63) / 11 + 1) % 3; }
constexpr int p1_time(int s) { int t = 0; for (int k = 0; k <= s; ++k) t += ((p1_sl(k) & 15) < 4 && p1_mt(k)) ? 70 : 35; return t; }
constexpr int TS = TSV;
constexpr int NB = NBV;
constexpr int p1_issued(int s) { int n = NB + (p1_time(s) + 34) / P1RATE; return n > P1_N ? P1_N : n; }
constexpr int p1_checked(int s) { int n = 1 + p1_time(s) / 84; return s >= 70 ? P1_N : n > P1_N ? P1_N : n; }
constexpr int EP_N = 87;
constexpr int ep_op(int k) {
  if (k == 86) return 43;
  const int o = k % 43;
  return o < 8 ? o : o == 8 ? 42 : o - 1;
}
constexpr int ep_weight(int k) { const int o = ep_op(k); return o == 42 ? 5 : o == 43 ? 4 : ((o >> 2) == 6 || (o >> 2) == 8) ? 2 : 1; }
constexpr int ep_done(int step) {
  if (step < 66) return 0;
  if (step >= 191) return 5 * EP_N;
  int total = 0; for (int k = 0; k < EP_N; ++k) total += ep_weight(k);
  const int budget = (5 * total * (step - 65)) / 126;
  int n = 0, c = 0;
  while (n < 5 * EP_N && c + ep_weight(n % EP_N) <= budget) { c += ep_weight(n % EP_N); ++n; }
  return n;
}
DI int sopaque(int v) { asm volatile("" : "+s"(v)); return v; }
DI int opaque(int v) { asm volatile("" : "+v"(v)); return v; }

__global__ void __launch_bounds__(512, 2) k_main(const unsigned short* __restrict__ x, unsigned short* __restrict__ out, unsigned char* __restrict__ ws) {
  extern __shared__ __attribute__((aligned(16))) unsigned char smem_[];
  LAS unsigned char* L = (LAS unsigned char*)smem_;
  const int b = blockIdx.x;
  const int g = ((b & 7) >> 1) * 4 + (b >> 6), j = (b & 1) * 8 + ((b >> 3) & 7);
  const int wv = __builtin_amdgcn_readfirstlane(threadIdx.x >> 6), lane = threadIdx.x & 63;
  const __amdgpu_buffer_rsrc_t rs = __builtin_amdgcn_make_buffer_rsrc(ws, 0, WS_END, 0x00020000);
  const int rh = j >> 3, cb = j & 7;
  const bool six = j < 6;
  STAMP_RT(62); STAMP(0);

  if (wv >= 5) {
    __builtin_amdgcn_s_setprio(LDPRIO);
    const int li = wv - 5;
    const unsigned w1 = W1_OFF + (j * 96 + 4 * li) * KB, w2 = W2_OFF + (cb * 64 + 4 * li) * KB, w3 = W3_OFF + (j * 192 + 4 * li) * KB, w4 = W4_OFF + (cb * 172 + li) * KB;
    const unsigned l16 = lane * 16;
    LAS unsigned char* sA = L + L_SLOT + 4 * li * KB;
    LAS unsigned char* sB = L + L_SLOT + 65536 + 4 * li * KB;
    v4u wr[59];
    auto wload = [&](auto B, auto N, unsigned src) { sfor<0, N.v>([&](auto I) { wr[B.v + I.v] = __builtin_amdgcn_raw_buffer_load_b128(rs, l16, src + I.v * 3 * KB, 0); }); };
    auto wput = [&](auto B, auto N, auto TOT, LAS unsigned char* dst) {
      sfor<0, N.v>([&](auto I) { if (3 * I.v + 2 < TOT.v || 3 * I.v + li < TOT.v) *(LAS v4u*)(dst - 3 * li * KB + I.v * 3 * KB + l16) = wr[B.v + I.v]; });
    };
    auto lpost = [&](unsigned v) { *(volatile LAS unsigned*)(L + L_FLG + wv * 4) = v; };
    dma_seg<48>(rs, w1, sA, li, l16);
    BAR();
    dma_seg<48>(rs, w1 + 48 * KB, sB, li, l16);
    sfor<0, 2>([&](auto LY) {
      constexpr int layer = LY.v;
      if constexpr (layer == 0) { WAITV(16); lpost(Z_W1); }
      WAITV(0); lpost(16 * layer + Z_W1B); STAMP(1 + 20 * layer);
      for (unsigned spins = 0; spins < (1u << 24); ++spins) {
        const v4u f = *(volatile LAS v4u*)(L + L_FLG);
        if (__builtin_amdgcn_readfirstlane(min(min(f.x, f.y), min(f.z, f.w)) >= 16u * layer + P_V)) break;
        __builtin_amdgcn_s_sleep(2);
      }
      asm volatile("" ::: "memory");
      dma_seg<64>(rs, w2, sA, li, l16);
      BAR();
      WAITV(0); lpost(16 * layer + Z_W2); STAMP(3 + 20 * layer);
      dma_seg<64>(rs, w3, sB, li, l16);
      STAMP(2 + 20 * layer);
      WAITV(0); lpost(16 * layer + Z_W3); STAMP(4 + 20 * layer);
      BAR();
      __builtin_amdgcn_s_sleep(D_S4);
      dma_seg<64>(rs, w3 + 64 * KB, sA, li, l16);
      WAITV(0); STAMP(5 + 20 * layer); BAR();
      dma_groups(rs, w3 + 128 * KB, sB, ((six ? 16 : 8) + 2 - li) / 3, l16);
      if constexpr (layer == 0) { wload(IC<0>{}, IC<22>{}, w4); WAITV(22); } else WAITV(0);
      STAMP(6 + 20 * layer); BAR();
      wput(IC<0>{}, IC<22>{}, IC<64>{}, sA);
      if constexpr (layer == 0) { wload(IC<22>{}, IC<22>{}, w4 + 64 * KB); wload(IC<44>{}, IC<15>{}, w4 + 128 * KB); }
      STAMP(7 + 20 * layer); BAR();
      wput(IC<22>{}, IC<22>{}, IC<64>{}, sB);
      STAMP(8 + 20 * layer); BAR();
      wput(IC<44>{}, IC<15>{}, IC<44>{}, sA);
      STAMP(9 + 20 * layer); BAR();
      if constexpr (layer == 0) dma_seg<48>(rs, w1, sB, li, l16);
      BAR();
      if constexpr (layer == 0) { WAITV(0); lpost(16 + Z_W1); __builtin_amdgcn_s_sleep(D_S4); dma_seg<48>(rs, w1 + 48 * KB, sA, li, l16); }
      LAS unsigned char* t = sA; sA = sB; sB = t;
    });
    STAMP_OUT();
    return;
  }

  if (wv == 4) {
    __builtin_amdgcn_s_setprio(SYPRIO);
    gu32* ctrl = (gu32*)(ws + CTRL_OFF);
    gu32* tmo = (gu32*)(ws + TMO_OFF);
    const int tb = (g + (lane >> 4)) * 16 + (lane & 15);
    auto add = [&](int hid, bool next) {
      if (lane < 16 || (next && lane < 32)) __hip_atomic_fetch_add(ctrl + (tb * 16 + hid) * 32, 1u, RLX_AGENT);
    };
    auto poll = [&](int hid, unsigned want) {
      gu32* w = ctrl + ((g * 16 + j) * 16 + hid) * 32;
      for (unsigned spins = 0;; ++spins) {
        if ((unsigned)__builtin_amdgcn_readfirstlane(__hip_atomic_load(w, RLX_AGENT)) >= want) break;
        if (spins > SPIN_CAP) { if (lane == 0) __hip_atomic_fetch_or(tmo, 0x10000u << hid, RLX_AGENT); break; }
        if (PSLEEP) __builtin_amdgcn_s_sleep(PSLEEP);
        if ((spins & 1023u) == 1023u && __builtin_amdgcn_readfirstlane(__hip_atomic_load(tmo, RLX_AGENT))) break;
      }
      __builtin_amdgcn_fence(__ATOMIC_ACQUIRE, "wavefront");
    };
    if (lane < 8) *(volatile LAS unsigned*)(L + L_FLG + lane * 4) = 0u;
    BAR();
    {
      v4u p[16], p2[2];
#pragma unroll
      for (int i = 0; i < 16; ++i) p[i] = __builtin_amdgcn_raw_buffer_load_b128(rs, lane * 16, CP_OFF + (j * 16 + i) * 1152, 0);
#pragma unroll
      for (int i = 0; i < 2; ++i) p2[i] = __builtin_amdgcn_raw_buffer_load_b128(rs, (lane >> 3) * 1152 + (lane & 7) * 16 + 1024, CP_OFF + (j * 16 + 8 * i) * 1152, 0);
      f32x4 a = __builtin_bit_cast(f32x4, p[0]);
#pragma unroll
      for (int i = 1; i < 16; ++i) a += __builtin_bit_cast(f32x4, p[i]);
      *(LAS f32x4*)(L + L_C1 + lane * 16) = a;
      f32x4 c = __builtin_bit_cast(f32x4, p2[0]) + __builtin_bit_cast(f32x4, p2[1]);
#pragma unroll
      for (int i = 0; i < 4; ++i) { c[i] = xsum32(xsum16(c[i])); c[i] += __shfl_xor(c[i], 8); }
      if (lane < 8) *(LAS f32x4*)(L + L_C1 + 1024 + lane * 16) = c;
    }
    const int tok = lane & 15, gq = lane >> 4, r = lane & 31, h = lane >> 5;
    auto row_sums = [&](auto NT, unsigned base, int t0, f32x2 (&sq)[5]) {
      for (unsigned tries = 0;; ++tries) {
        asm volatile("" ::: "memory");
        v4u p[NT.v][4];
        sfor<0, NT.v>([&](auto T) { sfor<0, 4>([&](auto C) { p[T.v][C.v] = ldg16(rs, lane * 16 + C.v * 1024, base + max(t0 + T.v, 0) * 4096); }); });
        bool odd = false;
        sfor<0, NT.v>([&](auto T) {
          float a[4];
          sfor<0, 4>([&](auto E) { a[E.v] = sadd(sadd(u2f(p[T.v][0][E.v]), u2f(p[T.v][1][E.v])), sadd(u2f(p[T.v][2][E.v]), u2f(p[T.v][3][E.v]))); });
          asm volatile("s_nop 1" : "+v"(a[0]), "+v"(a[1]), "+v"(a[2]), "+v"(a[3]));
          auto x = __builtin_amdgcn_permlane16_swap(f2u(a[0]), f2u(a[2]), false, false), y = __builtin_amdgcn_permlane16_swap(f2u(a[1]), f2u(a[3]), false, false);
          float xs = sadd(u2f(x[0]), u2f(x[1])), ys = sadd(u2f(y[0]), u2f(y[1]));
          asm volatile("s_nop 1" : "+v"(xs), "+v"(ys));
          auto z = __builtin_amdgcn_permlane32_swap(f2u(xs), f2u(ys), false, false);
          const float tot = sadd(u2f(z[0]), u2f(z[1]));
          odd |= tot != tot;
          auto b2 = __builtin_amdgcn_permlane32_swap(f2u(tot), f2u(tot), false, false);
          sq[T.v] = f32x2{u2f(b2[0]), u2f(b2[1])};
          SB();
        });
        if (__builtin_amdgcn_ballot_w64(odd) == 0) break;
        unsigned m = 0;
        sfor<0, NT.v>([&](auto T) { sfor<0, 4>([&](auto C) { m = chk(m, p[T.v][C.v]); }); });
        if (__builtin_amdgcn_ballot_w64(m == FILL) == 0) break;
        if (tries > RETRY_CAP) { if (lane == 0) __hip_atomic_fetch_or(tmo, 1u << NT.v, RLX_AGENT); break; }
      }
    };
    const int rtok = 2 * tok + (gq & 1);
    auto progress = [&](unsigned want) {
      for (unsigned spins = 0; spins < (1u << 24); ++spins) {
        const v4u f = *(volatile LAS v4u*)(L + L_FLG);
        if (min(min(f.x, f.y), min(f.z, f.w)) >= want) break;
        __builtin_amdgcn_s_sleep(1);
      }
      asm volatile("" ::: "memory");
    };
    auto post = [&](unsigned v) { asm volatile("" ::: "memory"); *(volatile LAS unsigned*)(L + L_FLG + 16) = v; };
#pragma unroll 1
    for (int layer = 0; layer < 2; ++layer) {
      if (layer) poll(6 * layer + H_X, g ? 24u : 16u);
      post(16 * layer + Y_X);
      STAMP(1 + 20 * layer);
      __builtin_amdgcn_s_sleep(STSLEEP);
      {
        f32x2 sq[5];
        row_sums(IC<5>{}, STA_OFF + layer * ST_SZ, 4 * g - 1, sq);
#pragma unroll
        for (int t = 0; t < 5; ++t) {
          float mean;
          float rstd = __builtin_amdgcn_rsqf(ln_var(sq[t].x, sq[t].y, mean));
          PIN(rstd);
          *(LAS float*)(L + L_ST + h * 640 + ((t ? t - 1 : 4) * 32 + rtok) * 4) = h ? -mean * rstd : rstd;
          SB();
        }
        post(16 * layer + Y_ST);
        STAMP(10 + 20 * layer);
      }
      {
        const float rstd = *(LAS float*)(L + L_ST + (144 + tok) * 4), mr = *(LAS float*)(L + L_ST + 640 + (144 + tok) * 4);
        const f32x4 r4 = *(LAS f32x4*)(L + L_ST + (144 + 4 * gq) * 4), m4 = *(LAS f32x4*)(L + L_ST + 640 + (144 + 4 * gq) * 4);
        float tk[2][4], tv[2][4];
#pragma unroll
        for (int e = 0; e < 2; ++e) {
          const f32x4 ck = *(LAS f32x4*)(L + L_C1 + (32 + 16 * e + 8 * (gq & 1) + 4 * (gq >> 1)) * 4);
          const float cv = *(LAS float*)(L + L_C1 + (64 + 16 * e + tok) * 4);
#pragma unroll
          for (int i = 0; i < 4; ++i) { tk[e][i] = smul(ck[i], mr); tv[e][i] = smul(m4[i], cv); PIN(tk[e][i]); PIN(tv[e][i]); }
        }
        progress(16 * layer + P_V);
        STAMP(11 + 20 * layer);
        f32x4 kp[2][4], vp[2][4];
#pragma unroll
        for (int e = 0; e < 2; ++e)
#pragma unroll
          for (int w = 0; w < 4; ++w) {
            kp[e][w] = *(LAS f32x4*)(L + L_EXC + w * 4096 + (gq & 1) * 1024 + (tok + 16 * e + 32 * (gq >> 1)) * 16);
            vp[e][w] = *(LAS f32x4*)(L + L_EXC + w * 4096 + 2048 + (gq >> 1) * 1024 + (16 * e + tok + 32 * (gq & 1)) * 16);
          }
        SB();
#pragma unroll
        for (int e = 0; e < 2; ++e) {
          f32x4 k4, v4;
#pragma unroll
          for (int i = 0; i < 4; ++i) {
            k4[i] = sfma(sadd(sadd(kp[e][0][i], kp[e][1][i]), sadd(kp[e][2][i], kp[e][3][i])), rstd, tk[e][i]);
            v4[i] = sfma(sadd(sadd(vp[e][0][i], vp[e][1][i]), sadd(vp[e][2][i], vp[e][3][i])), r4[i], tv[e][i]);
          }
          v4u o = {pk(k4[0], k4[1]), pk(k4[2], k4[3]), pk(v4[0], v4[1]), pk(v4[2], v4[3])};
          if (!g) o = v4u{0, 0, 0, 0};
          *(LAS v4u*)(L + L_KV + e * 1024 + lane * 16) = o;
        }
      }
      STAMP(2 + 20 * layer);
      BAR();
      progress(16 * layer + P_O);
      if (D_O) __builtin_amdgcn_s_sleep(D_O);
      STAMP(3 + 20 * layer);
      add(6 * layer + H_O, false);
      poll(6 * layer + H_O, 16u);
      post(16 * layer + Y_O);
      STAMP(4 + 20 * layer);
      progress(16 * layer + P_X1);
      STAMP(5 + 20 * layer);
      add(6 * layer + H_X1, false);
      BAR();
      poll(6 * layer + H_X1, 16u);
      post(16 * layer + Y_X1);
      STAMP(6 + 20 * layer);
      __builtin_amdgcn_s_sleep(STSLEEP);
      {
        f32x2 sq[5];
        row_sums(IC<4>{}, STB_OFF + layer * ST_SZ, 4 * g, sq);
#pragma unroll
        for (int t = 0; t < 4; ++t) {
          float mean;
          float vv = ln_var(sq[t].x, sq[t].y, mean), rstd = __builtin_amdgcn_rsqf(vv);
          PIN(rstd);
          constexpr float c2 = -2.f * 0.7978845608028654f * 1.4426950408889634f, c1 = c2 * 0.044715f;
          float k1 = c1 * rstd; PIN(k1);
          float k2 = c2 * rstd; PIN(k2);
          float r2 = rstd * rstd; PIN(r2);
          k1 *= r2; PIN(k1);
          if (h == 0) *(LAS f32x4*)(L + L_ST + (t * 32 + rtok) * 16) = f32x4{-mean, k1, k2, vv};
          SB();
        }
      }
      STAMP(12 + 20 * layer);
      BAR();
      BAR();
      progress(16 * layer + P_H0);
      add(6 * layer + H_H0, false);
      {
        gu32* c0 = ctrl + ((g * 16 + j) * 16 + 6 * layer + H_H0) * 32;
        bool seen = false;
        for (unsigned spins = 0; spins < (1u << 22); ++spins) {
          if (!seen && (unsigned)__builtin_amdgcn_readfirstlane(__hip_atomic_load(c0, RLX_AGENT)) >= 16u) {
            seen = true; __builtin_amdgcn_fence(__ATOMIC_ACQUIRE, "wavefront"); post(16 * layer + Y_H0);
          }
          const v4u f = *(volatile LAS v4u*)(L + L_FLG);
          if (min(min(f.x, f.y), min(f.z, f.w)) >= 16u * layer + P_H1) break;
          if (seen) __builtin_amdgcn_s_sleep(1);
        }
        asm volatile("" ::: "memory");
        add(6 * layer + H_H1, false);
        STAMP(7 + 20 * layer);
        BAR();
        if (!seen) { poll(6 * layer + H_H0, 16u); post(16 * layer + Y_H0); }
        STAMP(8 + 20 * layer);
      }
      poll(6 * layer + H_H1, 16u);
      post(16 * layer + Y_H1);
      STAMP(13 + 20 * layer);
      progress(16 * layer + P_H2);
      add(6 * layer + H_H2, false);
      STAMP(14 + 20 * layer);
      BAR();
      poll(6 * layer + H_H2, 16u);
      post(16 * layer + Y_H2);
      STAMP(15 + 20 * layer);
      BAR();
      if (layer == 0) {
        progress(P_X2);
        STAMP(9 + 20 * layer);
        add(6 + H_X, rh == 1 && g < 15);
      }
      BAR();
    }
    STAMP_OUT();
    return;
  }

#define PHASE_SCALARS() const int w = sopaque(wv), g = sopaque(g0), j = sopaque(j0), rh = j >> 3, cb = j & 7, tt = w & 1, kh = w >> 1, t32a = 4 * g + w, t32b = 4 * g + 2 * rh + tt, cbk = 2 * cb + kh; \
  const bool six = j < 6; (void)t32a; (void)t32b; (void)cbk; (void)six
  const int g0 = g, j0 = j;
  unsigned xr[8];
  {
    PHASE_SCALARS();
#pragma unroll
    for (int q = 0; q < 4; ++q) { v2u v = *(const v2u*)(x + (size_t)(32 * t32b + (lane & 31)) * D_ + 32 * cbk + 8 * q + 4 * (lane >> 5)); xr[2 * q] = v.x; xr[2 * q + 1] = v.y; }
  }
#define PHASE_LANE() PHASE_SCALARS(); const int lane = opaque(lane0), tok = lane & 15, gq = lane >> 4, r = lane & 31, h = lane >> 5; const unsigned l16 = lane * 16, sA = L_SLOT + l16 + layer * 65536, sB = L_SLOT + l16 + (layer ^ 1) * 65536; (void)tok; (void)gq; (void)r; (void)h; (void)sA; (void)sB
  const int lane0 = lane;
  unsigned redo = 0;
  auto again = [&](unsigned m, unsigned& tries) {
    if (__builtin_expect(__builtin_amdgcn_ballot_w64(m == FILL) == 0, 1)) return false;
    ++redo;
    if (++tries > RETRY_CAP) { __builtin_amdgcn_raw_buffer_store_b32(1u, rs, 0, TMO_OFF, 16); return false; }
    return true;
  };
  auto mark = [&](unsigned v) { asm volatile("" ::: "memory"); *(volatile LAS unsigned*)(L + L_FLG + wv * 4) = v; };
  auto await = [&](unsigned f, unsigned want) {
    for (unsigned spins = 0; (unsigned)__builtin_amdgcn_readfirstlane(f) < want && spins < (1u << 24); ++spins) f = *(volatile LAS unsigned*)(L + L_FLG + 16);
    asm volatile("" ::: "memory");
  };
  auto start = [&](unsigned y, unsigned z) {
    for (unsigned spins = 0; spins < (1u << 24); ++spins) {
      const v4u f = *(volatile LAS v4u*)(L + L_FLG + 16);
      if (__builtin_amdgcn_readfirstlane(f.x >= y && min(min(f.y, f.z), f.w) >= z)) break;
      if (SSLEEP) __builtin_amdgcn_s_sleep(SSLEEP);
    }
    asm volatile("" ::: "memory");
  };
  BAR();
  constexpr int PD = PDV, GW = 4;

#pragma unroll 1
  for (int layer = 0; layer < 2; ++layer) {
    {
      PHASE_LANE();
      const unsigned xb = XA_OFF + layer * X_SZ + t32a * 32 * KB;
      const unsigned hb = XA_OFF + layer * X_SZ + (g ? 4 * g - 1 : 0) * 32 * KB;
      const unsigned l16h = r >= 16 ? l16 : l16 + 256 + KB;
      auto ksof = [&](int i) { return 16 * (i >> 4) + ((i + 4 * w) & 15); };
      f32x4 bc, bp;
#pragma unroll
      for (int i = 0; i < 4; ++i) {
        const int d = tok - (4 * gq + i);
        bc[i] = d >= 0 ? 0.f : -__builtin_inff();
        bp[i] = d <= -2 ? 0.f : -__builtin_inff();
      }
      const bool par = tok & 1;
      unsigned sel0 = par ? 0x01000c0cu : 0x0c0c0100u, sel1 = par ? 0x03020c0cu : 0x0c0c0302u;
      PIN(bc); PIN(bp); PIN(sel0); PIN(sel1);
      v4u xa[32], xh[8], wq[96];
      auto item = [&](auto N) -> v4u& { constexpr int i = p1_slot(N.v); if constexpr (N.v == p1_items(i)) return xa[i]; else return xh[(i >> 4) * 4 + (i & 2)]; };
      auto request = [&](auto N) {
        constexpr int i = p1_slot(N.v);
        if constexpr (N.v == p1_items(i)) item(N) = ldg16(rs, l16, xb + ksof(i) * KB); else item(N) = ldg16(rs, l16h, hb + ksof(i) * KB);
      };
      f32x16 aq, ak, av, hk, hv;
      f32x4 cc[2], r4[4];
      const unsigned bst = opaque(L_ST + w * 128 + r * 4), bs4 = opaque(L_ST + w * 128 + 16 * h), bc1 = opaque(L_C1 + 16 * h);
      const unsigned bex = opaque(L_EXC + w * 4096 + l16), bkv = opaque(L_KV + w * 2048 + l16), bcv = opaque(L_C1 + (64 + r) * 4);
      float rstd, mr, rsq, mrq, cv, t[48], ln[4];
      unsigned sflag, pflag, hc[8], kd[8], vd[8], m, tries = 0;
      constexpr float cs = 0.25f * 1.4426950408889634f;
      auto p1_addr = [&](auto I) { constexpr int i = p1_sl(I.v); return (i < 16 ? sA : sB) + (ksof(i) & 15) * 3 * KB + p1_mt(I.v) * KB; };
      auto p1_mma = [&](auto I, v4u wf) {
        constexpr int i = p1_sl(I.v), mt = p1_mt(I.v);
        if constexpr (mt == 0) mma32<i == 0>(aq, wf, xa[i]);
        if constexpr (mt == 1) mma32<i == 0>(ak, wf, xa[i]);
        if constexpr (mt == 2) mma32<i == 0>(av, xa[i], wf);
        if constexpr (mt == 1 && (i & 15) < 4) mma32<i == 0>(hk, wf, xh[(i >> 4) * 4 + (i & 3)]);
        if constexpr (mt == 2 && (i & 15) < 4) mma32<i == 0>(hv, xh[(i >> 4) * 4 + (i & 3)], wf);
      };
      auto swap16 = [](unsigned& x, unsigned& y) { auto z = __builtin_amdgcn_permlane16_swap(x, y, false, false); x = z[0]; y = z[1]; };
      auto swap32 = [](unsigned& x, unsigned& y) { auto z = __builtin_amdgcn_permlane32_swap(x, y, false, false); x = z[0]; y = z[1]; };
      auto p1_extra = [&](auto I) {
        constexpr int s = I.v;
        if constexpr (s == 0) STAMP(43 + 10 * layer);
        sfor<(s ? p1_issued(s - 1) : NB), p1_issued(s)>([&](auto N) { request(N); });
        sfor<(s ? p1_checked(s - 1) : 0), p1_checked(s)>([&](auto N) { m = chk(m, item(N)); PIN(m); });
        if constexpr (s < 63 && s % 3 == 2 && ((s / 3) & 13) == 0) {
          constexpr int a = ((s / 3) >> 4) * 4 + ((s / 3) & 2);
          sfor<0, 4>([&](auto C) { auto x = __builtin_amdgcn_permlane16_swap(xh[a][C.v], xh[a][C.v], false, false); xh[a + 1][C.v] = x[0]; });
          PIN(xh[a + 1]);
        }
        if constexpr (s == 1) STAMP(40 + 10 * layer);
        if constexpr (s == 47) STAMP(44 + 10 * layer);
        if constexpr (s == 60) STAMP(45 + 10 * layer);
        if constexpr (s == 70) STAMP(47 + 10 * layer);
        if constexpr (s == 84) STAMP(48 + 10 * layer);
        if constexpr (s == 48 - PD - 1) start(0, 16 * layer + Z_W1B);
        if constexpr (s == TS - 6) sflag = *(volatile LAS unsigned*)(L + L_FLG + 16);
        if constexpr (s == TS - 3) {
          await(sflag, 16 * layer + Y_ST);
          rstd = *(LAS float*)(L + bst); mr = *(LAS float*)(L + bst + 640); cv = *(LAS float*)(L + bcv);
        }
        if constexpr (s >= TS && s < TS + 24) {
          sfor<2 * (s - TS), 2 * (s - TS) + 2>([&](auto V) { constexpr int v = V.v; t[v] = cc[(v >> 2) & 1][v & 3] * (v < 16 ? mr : v < 32 ? cv : mrq); PIN(t[v]); });
        }
        if constexpr (s == TS + 1) { rsq = rstd * cs; mrq = mr * cs; PIN(rsq); PIN(mrq); }
        if constexpr (s >= TS - 3 && s < TS + 21 && ((s - TS) & 1)) {
          constexpr int u = (s - TS + 3) / 2;
          if constexpr (u < 4) cc[u & 1] = *(LAS f32x4*)(L + bc1 + 128 + 32 * u);
          else if constexpr (u < 8) cc[u & 1] = *(LAS f32x4*)(L + bs4 + 640 + 32 * (u - 4));
          else cc[u & 1] = *(LAS f32x4*)(L + bc1 + 32 * (u - 8));
        }
        if constexpr (s == 56) pflag = *(volatile LAS unsigned*)(L + L_FLG + (w ^ 2) * 4);
        if constexpr (s == 61) {
          for (unsigned spins = 0; (unsigned)__builtin_amdgcn_readfirstlane(pflag) + (16 - P_EX) < 16u * layer && spins < (1u << 24); ++spins) pflag = *(volatile LAS unsigned*)(L + L_FLG + (w ^ 2) * 4);
          asm volatile("" ::: "memory");
          PIN(hk); PIN(hv);
        }
        if constexpr (s >= 62 && s < 70) { constexpr int q = s - 62; auto x = __builtin_amdgcn_permlane16_swap(f2u(hk[q]), f2u(hk[q + 8]), false, false); hc[q] = x[1]; PIN(hc[q]); }
        if constexpr (s == 62 || s == 64) *(LAS f32x4*)(L + bex + (s / 2 - 29) * 1024) = f32x4{hv[2 * s - 116], hv[2 * s - 115], hv[2 * s - 114], hv[2 * s - 113]};
        if constexpr (s == 67) *(LAS v4u*)(L + bex) = v4u{hc[0], hc[1], hc[2], hc[3]};
        if constexpr (s == 70) *(LAS v4u*)(L + bex + 1024) = v4u{hc[4], hc[5], hc[6], hc[7]};
        if constexpr (s >= 77 && s < 85 && (s & 1)) r4[(s - 77) >> 1] = *(LAS f32x4*)(L + bs4 + 16 * (s - 77));
        if constexpr (s >= 76 && s < 84) {
          constexpr int i = s - 76;
          ln[0] = fmaf(ak[2 * i], rstd, t[2 * i]); PIN(ln[0]); ln[1] = fmaf(ak[2 * i + 1], rstd, t[2 * i + 1]); PIN(ln[1]);
          if constexpr (i > 0) { kd[i - 1] = pk(ln[2], ln[3]); PIN(kd[i - 1]); }
          ln[2] = ln[0]; ln[3] = ln[1];
        }
        if constexpr (s == 84) { kd[7] = pk(ln[2], ln[3]); swap16(kd[0], kd[2]); swap16(kd[1], kd[3]); PIN(kd[0]); PIN(kd[1]); PIN(kd[2]); PIN(kd[3]); PIN(kd[7]); }
        if constexpr (s == 85) { swap16(kd[4], kd[6]); swap16(kd[5], kd[7]); PIN(kd[4]); PIN(kd[5]); PIN(kd[6]); PIN(kd[7]); }
        if constexpr (s >= 87 && s < 95) {
          constexpr int i = (s - 87) ^ 4, ip = (s - 88) ^ 4;
          ln[0] = fmaf(av[2 * i], r4[i >> 1][2 * (i & 1)], t[16 + 2 * i]); PIN(ln[0]); ln[1] = fmaf(av[2 * i + 1], r4[i >> 1][2 * (i & 1) + 1], t[16 + 2 * i + 1]); PIN(ln[1]);
          if constexpr (s > 87) { vd[ip] = pk(ln[2], ln[3]); PIN(vd[ip]); }
          ln[2] = ln[0]; ln[3] = ln[1];
          if constexpr (s == 92) { swap32(vd[4], vd[6]); PIN(vd[4]); PIN(vd[6]); }
          if constexpr (s == 93) { swap32(vd[5], vd[7]); PIN(vd[5]); PIN(vd[7]); }
          if constexpr (s == 94) { swap16(vd[4], vd[6]); PIN(vd[4]); PIN(vd[6]); }
        }
        if constexpr (s == 95) {
          swap16(vd[5], vd[7]); vd[3] = pk(ln[2], ln[3]); PIN(vd[3]);
          *(LAS v4u*)(L + bkv + 2048) = v4u{kd[2], kd[3], vd[4], vd[5]};
          *(LAS v4u*)(L + bkv + 3072) = v4u{kd[6], kd[7], vd[6], vd[7]};
        }
      };
      do {
        m = 0;
        SB();
        start(16 * layer + Y_X, 16 * layer + Z_W1);
        STAMP(1 + 20 * layer);
        request(IC<0>{});
        wfirst<PD>(wq, L, p1_addr);
        paced<0, NB>([&](auto N) { if constexpr (N.v) request(N); });
        STAMP(42 + 10 * layer);
        wstream<0, 71, 96, PD, 3, -1, -1>(wq, L, p1_addr, p1_mma, p1_extra);
      } while (again(m, tries));
      mark(16 * layer + P_V);
      wstream<71, 96, 96, PD, 3, -1, -1>(wq, L, p1_addr, p1_mma, p1_extra);
      swap32(vd[0], vd[2]); swap32(vd[1], vd[3]);
      SB();
      asm volatile("s_nop 7");
      swap16(vd[0], vd[2]); swap16(vd[1], vd[3]);
      SB();
      STAMP(2 + 20 * layer);
      unsigned qd[8];
      {
        float y[16];
        sfor<0, 16>([&](auto I) { y[I.v] = fmaf(aq[I.v], rsq, t[32 + I.v]); PIN(y[I.v]); });
        SB();
        sfor<0, 8>([&](auto I) { qd[I.v] = pk(y[2 * I.v], y[2 * I.v + 1]); });
        SB();
        swap16(qd[0], qd[2]); swap16(qd[1], qd[3]); swap16(qd[4], qd[6]); swap16(qd[5], qd[7]);
      }
      BAR();
      STAMP(16 + 20 * layer);
      v2u kp[2], vp[2];
      sfor<0, 2>([&](auto E) { kp[E.v] = *(LAS v2u*)(L + bkv + E.v * 1024); vp[E.v] = *(LAS v2u*)(L + bkv + E.v * 1024 + 8); });
      f32x4 sc[4], sp[4];
      sfor<0, 2>([&](auto E) {
        constexpr int e = E.v;
        sc[2 + e] = mfma16k16(v2u{kd[4 * e + 2], kd[4 * e + 3]}, v2u{qd[4 * e + 2], qd[4 * e + 3]}, bc);
        sp[2 + e] = mfma16k16(v2u{kd[4 * e], kd[4 * e + 1]}, v2u{qd[4 * e + 2], qd[4 * e + 3]}, bp);
      });
      sfor<0, 2>([&](auto E) { constexpr int e = E.v; sc[e] = mfma16k16(v2u{kd[4 * e], kd[4 * e + 1]}, v2u{qd[4 * e], qd[4 * e + 1]}, bc); });
      SB();
      if (t32a == 0) { bp = f32x4{-__builtin_inff(), -__builtin_inff(), -__builtin_inff(), -__builtin_inff()}; PIN(bp); }
      sfor<0, 2>([&](auto E) { constexpr int e = E.v; sp[e] = mfma16k16(kp[e], v2u{qd[4 * e], qd[4 * e + 1]}, bp); });
      SB();
      float a[4][4], mx[4];
      sfor<0, 4>([&](auto C) {
        constexpr int c = C.v ^ 2;
        a[c][2] = pick(par, sc[c][0], sc[c][1]); a[c][3] = pick(par, sc[c][2], sc[c][3]);
        a[c][0] = pick(par, sp[c][0], sp[c][1]); a[c][1] = pick(par, sp[c][2], sp[c][3]);
        SB();
      });
      sfor<0, 4>([&](auto C) { mx[C.v ^ 2] = vmax3(a[C.v ^ 2][0], a[C.v ^ 2][1], a[C.v ^ 2][2]); });
      SB();
      sfor<0, 4>([&](auto C) { mx[C.v ^ 2] = vmax(mx[C.v ^ 2], a[C.v ^ 2][3]); });
      SB();
      {
        unsigned x0 = f2u(mx[0]), x1 = f2u(mx[1]), x2 = f2u(mx[2]), x3 = f2u(mx[3]);
        asm volatile("s_nop 1" : "+v"(x0), "+v"(x1), "+v"(x2), "+v"(x3));
        swap16(x2, x3); swap16(x0, x1);
        unsigned y0 = f2u(vmax(u2f(x0), u2f(x1))), y1 = f2u(vmax(u2f(x2), u2f(x3)));
        asm volatile("s_nop 1" : "+v"(y0), "+v"(y1));
        swap32(y0, y1);
        unsigned z0 = f2u(vmax(u2f(y0), u2f(y1))), z1 = z0;
        swap32(z0, z1);
        unsigned z2 = z0, z3 = z1;
        swap16(z0, z2); swap16(z1, z3);
        mx[0] = u2f(z0); mx[1] = u2f(z2); mx[2] = u2f(z1); mx[3] = u2f(z3);
      }
      SB();
      sfor<0, 16>([&](auto I) { a[(I.v >> 2) ^ 2][I.v & 3] -= mx[(I.v >> 2) ^ 2]; PIN(a[(I.v >> 2) ^ 2][I.v & 3]); });
      SB();
      if constexpr (A_O == 2) mark(16 * layer + P_O);
      const f32x4 z4 = {0, 0, 0, 0};
      const v4u ones = {0x3f803f80u, 0x3f803f80u, 0x3f803f80u, 0x3f803f80u};
      f32x4 o[4], l[4];
      sfor<0, 2>([&](auto H) {
        constexpr int c0 = 2 - 2 * H.v;
        sfor<0, 8>([&](auto I) { a[c0 + (I.v >> 2)][I.v & 3] = __builtin_amdgcn_exp2f(a[c0 + (I.v >> 2)][I.v & 3]); });
        SB();
        unsigned pc[4];
        sfor<0, 4>([&](auto I) { pc[I.v] = pk(a[c0 + (I.v >> 1)][2 * (I.v & 1)], a[c0 + (I.v >> 1)][2 * (I.v & 1) + 1]); });
        SB();
        v4u pf[2];
        sfor<0, 2>([&](auto E) {
          pf[E.v] = v4u{__builtin_amdgcn_perm(0u, pc[2 * E.v], sel0), __builtin_amdgcn_perm(0u, pc[2 * E.v], sel1), __builtin_amdgcn_perm(0u, pc[2 * E.v + 1], sel0), __builtin_amdgcn_perm(0u, pc[2 * E.v + 1], sel1)};
        });
        SB();
        sfor<0, 2>([&](auto E) {
          constexpr int e = E.v;
          l[c0 + e] = mfma16(ones, pf[e], z4);
          o[c0 + e] = mfma16(c0 ? v4u{vd[2 * e], vd[2 * e + 1], vd[4 + 2 * e], vd[5 + 2 * e]} : v4u{vp[e].x, vp[e].y, vd[2 * e], vd[2 * e + 1]}, pf[e], z4);
        });
        SB();
      });
      if constexpr (A_O == 1) mark(16 * layer + P_O);
      const unsigned ov = ((gq & 1) * 64 + tok + 32 * (gq >> 1)) * 16, os = O32_OFF + layer * X_SZ + (t32a * 32 + 2 * j) * KB;
      sfor<0, 2>([&](auto H) {
        constexpr int c0 = 2 - 2 * H.v;
        float il[2], y[8];
        sfor<0, 2>([&](auto E) { il[E.v] = __builtin_amdgcn_rcpf(l[c0 + E.v][0]); });
        sfor<0, 8>([&](auto I) { y[I.v] = o[c0 + (I.v >> 2)][I.v & 3] * il[I.v >> 2]; PIN(y[I.v]); });
        SB();
        unsigned d[4];
        sfor<0, 4>([&](auto I) { d[I.v] = nf(pk(y[2 * I.v], y[2 * I.v + 1])); });
        swap16(d[0], d[2]); swap16(d[1], d[3]);
        stg16(v4u{d[0], d[1], d[2], d[3]}, rs, ov + c0 * 128, os);
        SB();
      });
      if constexpr (A_O == 0) mark(16 * layer + P_O);
      STAMP(3 + 20 * layer);
    }
    {
      PHASE_LANE();
      f32x16 acc[2], seed;
#pragma unroll
      for (int i = 0; i < 8; ++i) { seed[2 * i] = blo(xr[i]); seed[2 * i + 1] = bhi(xr[i]); }
      PIN(seed);
      const unsigned ob = O32_OFF + layer * X_SZ + (t32b * 32 + 16 * kh) * KB;
      const unsigned s0 = sA + kh * 32 * KB + kh * KB, s1 = sA + kh * 32 * KB + (kh ^ 1) * KB;
      v4u of[16], wq[32];
      auto p2_addr = [&](auto I) { return ((I.v & 1) ? s1 : s0) + (I.v >> 1) * 2 * KB; };
      unsigned m, tries = 0;
      do {
        m = 0;
        SB();
        start(16 * layer + Y_O, 16 * layer + Z_W2);
        STAMP(4 + 20 * layer);
        of[0] = ldg16(rs, l16, ob);
        wfirst<PD>(wq, L, p2_addr);
        paced<0, NB>([&](auto I) { if constexpr (I.v) of[I.v] = ldg16(rs, l16 + (I.v & 3) * KB, ob + (I.v >> 2) * 4 * KB); });
        wstream<0, 32, 32, PD, GW, -1, -1>(wq, L, p2_addr,
          [&](auto I, v4u wf) { if constexpr (I.v == 0) mma32c(acc[0], wf, of[0], seed); else mma32<I.v == 1>(acc[I.v & 1], wf, of[I.v >> 1]); },
          [&](auto I) {
            constexpr int n = (I.v >> 1) + NB;
            if constexpr (!(I.v & 1) && n < 16) of[n] = ldg16(rs, l16 + (n & 3) * KB, ob + (n >> 2) * 4 * KB);
            if constexpr (!(I.v & 1)) { m = chk(m, of[I.v >> 1]); PIN(m); }
            if constexpr (I.v == A_X1) mark(16 * layer + P_X1);
            if constexpr (I.v == 0) STAMP(49 + 10 * layer);
          });
      } while (again(m, tries));
      MFMA_DONE();
      SB();
      STAMP(5 + 20 * layer);
#pragma unroll
      for (int q = 0; q < 4; ++q) *(LAS f32x4*)(L + L_EXC + (w * 4 + q) * 1024 + l16) = f32x4{acc[1][4 * q], acc[1][4 * q + 1], acc[1][4 * q + 2], acc[1][4 * q + 3]};
      BAR();
      f32x4 p[4];
#pragma unroll
      for (int q = 0; q < 4; ++q) p[q] = *(LAS f32x4*)(L + L_EXC + ((w ^ 2) * 4 + q) * 1024 + l16);
      SB();
#pragma unroll
      for (int c = 0; c < 2; ++c) {
#pragma unroll
        for (int q = 2 * c; q < 2 * c + 2; ++q) {
          xr[2 * q] = nf(pk(acc[0][4 * q] + p[q][0], acc[0][4 * q + 1] + p[q][1]));
          xr[2 * q + 1] = nf(pk(acc[0][4 * q + 2] + p[q][2], acc[0][4 * q + 3] + p[q][3]));
        }
        stg16(v4u{xr[4 * c], xr[4 * c + 1], xr[4 * c + 2], xr[4 * c + 3]}, rs, l16 + c * KB, X32_OFF + layer * X_SZ + (t32b * 32 + 2 * cbk) * KB);
        SB();
      }
      const f32x2 st = block_stats(xr);
      if (h == 0) stg8(v2u{nf(f2u(st.x)), nf(f2u(st.y))}, rs, r * 8, STB_OFF + layer * ST_SZ + (t32b * 16 + cbk) * 256);
#pragma unroll
      for (int c = 0; c < 2; ++c) *(LAS v4u*)(L + L_KV + (w + 1) * 2048 + c * 1024 + l16) = v4u{xr[4 * c], xr[4 * c + 1], xr[4 * c + 2], xr[4 * c + 3]};
      STAMP(6 + 20 * layer);
    }
    {
      PHASE_LANE();
      const unsigned xb = X32_OFF + layer * X_SZ + t32a * 32 * KB;
      const unsigned hb = H32_OFF + layer * H32_SZ + t32a * 86 * KB;
      f32x4 cg, cu;
      cg = *(LAS f32x4*)(L + L_C3 + (4 * h) * 4); cu = *(LAS f32x4*)(L + L_C3 + (16 + 4 * h) * 4);
      v4u xa[32], wq[192];
      unsigned m, tries = 0;
      float nmean, k1, k2, vv;
      float ea[4], eb[4], et[4];
      unsigned eo[4];
      f32x16 ac[3];
      const unsigned bc3 = opaque(L_C3 + 16 * h);
      auto ep = [&](auto K) {
        constexpr int lu = K.v / EP_N, k = K.v % EP_N, q = k / 43, o = ep_op(k), e = o & 3, sg = o >> 2, i0 = 4 * q + e;
        if constexpr (o == 42) {
          constexpr int off = q == 0 ? lu * 128 + 32 : (lu + 1) * 128;
          cg = *(LAS f32x4*)(L + bc3 + off); cu = *(LAS f32x4*)(L + bc3 + off + 64);
        } else if constexpr (o == 43) stg16(v4u{eo[0], eo[1], eo[2], eo[3]}, rs, l16, hb + hpos(j, lu) * KB);
        else if constexpr (sg == 0) { ea[e] = fmaf(cg[e], nmean, ac[lu % 3][i0]); PIN(ea[e]); }
        else if constexpr (sg == 1) { eb[e] = fmaf(cu[e], nmean, ac[lu % 3][8 + i0]); PIN(eb[e]); }
        else if constexpr (sg == 2) { et[e] = ea[e] * ea[e]; PIN(et[e]); }
        else if constexpr (sg == 3) { et[e] = fmaf(et[e], k1, k2); PIN(et[e]); }
        else if constexpr (sg == 4) { et[e] *= ea[e]; PIN(et[e]); }
        else if constexpr (sg == 5) { eb[e] *= ea[e]; PIN(eb[e]); }
        else if constexpr (sg == 6) { et[e] = __builtin_amdgcn_exp2f(et[e]); PIN(et[e]); }
        else if constexpr (sg == 7) { et[e] = fmaf(et[e], vv, vv); PIN(et[e]); }
        else if constexpr (sg == 8) { et[e] = __builtin_amdgcn_rcpf(et[e]); PIN(et[e]); }
        else if constexpr (sg == 9) { eb[e] *= et[e]; PIN(eb[e]); }
        else { eo[2 * q + e] = pk(eb[2 * e], eb[2 * e + 1]); PIN(eo[2 * q + e]); }
        SB();
      };
      auto p3_addr = [&](auto I) { return (I.v >= 64 && I.v < 128 ? sA : sB) + (I.v & 63) * KB; };
      auto p3_mma = [&](auto I, v4u wf) {
        constexpr int i = I.v;
        if constexpr (i < 64) mma32<(i < 2)>(ac[i & 1], wf, xa[i >> 1]);
        else mma32<(i & 31) == 0>(ac[(i >> 5) % 3], wf, xa[i & 31]);
      };
      constexpr int E0 = 64 + 2 - PD;
      auto p3_extra = [&](auto I) {
        constexpr int i = I.v;
        if constexpr (i < 64) {
          constexpr int n = (i >> 1) + NB;
          if constexpr (!(i & 1) && n < 32) xa[n] = ldg16(rs, l16 + (n & 3) * KB, xb + (n >> 2) * 4 * KB);
          if constexpr (i < E0) sfor<(i * 32) / E0, ((i + 1) * 32) / E0>([&](auto N) { m = chk(m, xa[N.v]); PIN(m); });
          if constexpr (i == 62) { const f32x4 sv = *(LAS f32x4*)(L + L_ST + (w * 32 + r) * 16); nmean = sv[0]; k1 = sv[1]; k2 = sv[2]; vv = sv[3]; }
          if constexpr (i == E0 - 1) STAMP(8 + 20 * layer);
        } else {
          if constexpr (i == 141) { static_assert(ep_done(120) >= 2 * EP_N && ep_done(140) < 3 * EP_N, ""); WAITV(0); mark(16 * layer + P_H0); }
          if constexpr (i == 189) { static_assert(ep_done(168) >= 4 * EP_N && ep_done(188) < 5 * EP_N, ""); WAITV(0); mark(16 * layer + P_H1); }
          sfor<ep_done(i - 1), ep_done(i)>(ep);
          if constexpr (i == E0 + 63) STAMP(9 + 20 * layer);
        }
      };
      static_assert(ep_done(95) >= 52 && ep_done(127) >= EP_N + 52 && ep_done(159) >= 2 * EP_N + 52, "an accumulator is read out before it is reused");
      static_assert(ep_done(98) <= 2 * EP_N && ep_done(130) <= 3 * EP_N && ep_done(162) <= 4 * EP_N, "an accumulator is complete before it is read");
      do {
        m = 0;
        SB();
        start(16 * layer + Y_X1, 16 * layer + Z_W3);
        STAMP(7 + 20 * layer);
        xa[0] = ldg16(rs, l16, xb);
        wfirst<PD>(wq, L, p3_addr);
        paced<0, NB>([&](auto I) { if constexpr (I.v) xa[I.v] = ldg16(rs, l16 + (I.v & 3) * KB, xb + (I.v >> 2) * 4 * KB); });
        wstream<0, E0, 192, PD, GW, 64, 128>(wq, L, p3_addr, p3_mma, p3_extra);
      } while (again(m, tries));
      wstream<E0, 192, 192, PD, GW, 64, 128>(wq, L, p3_addr, p3_mma, p3_extra);
      MFMA_DONE();
      SB();
      STAMP(10 + 20 * layer);
      BAR();
      await(*(volatile LAS unsigned*)(L + L_FLG + 16), 16 * layer + Y_H0);
      STAMP(16 + 20 * layer + 30 - 10 * layer);
      const unsigned hb4 = H32_OFF + layer * H32_SZ + (t32b * 86 + 43 * kh) * KB;
      v4u hf[43];
      sfor<0, 6>([&](auto I) { hf[I.v] = ldg16(rs, l16 + (I.v & 3) * KB, hb4 + (I.v >> 2) * 4 * KB); SB(); });
      {
        const f32x4 cg2 = *(LAS f32x4*)(L + L_C3 + (5 * 32 + 8 + 4 * h) * 4), cu2 = *(LAS f32x4*)(L + L_C3 + (5 * 32 + 24 + 4 * h) * 4);
        float v[8];
#pragma unroll
        for (int i = 0; i < 8; ++i) {
          const float a = fmaf(i < 4 ? cg[i & 3] : cg2[i & 3], nmean, ac[2][i]), bb = fmaf(i < 4 ? cu[i & 3] : cu2[i & 3], nmean, ac[2][8 + i]);
          v[i] = (a * bb) * __builtin_amdgcn_rcpf(fmaf(__builtin_amdgcn_exp2f(a * fmaf(a * a, k1, k2)), vv, vv));
        }
        stg16(v4u{pk(v[0], v[1]), pk(v[2], v[3]), pk(v[4], v[5]), pk(v[6], v[7])}, rs, six ? l16 : 0x40000000u, hb + hpos(j, 5) * KB);
      }
      SB();
      paced<6, 14>([&](auto I) { hf[I.v] = ldg16(rs, l16 + (I.v & 3) * KB, hb4 + (I.v >> 2) * 4 * KB); });
      STAMP(11 + 20 * layer);
      f32x16 acc[2];
#pragma unroll
      for (int c = 0; c < 2; ++c) {
        const v4u t = *(LAS v4u*)(L + L_KV + (w + 1) * 2048 + c * 1024 + l16);
#pragma unroll
        for (int i = 0; i < 4; ++i) { acc[0][8 * c + 2 * i] = blo(t[i]); acc[0][8 * c + 2 * i + 1] = bhi(t[i]); }
      }
      PIN(acc[0]);
      asm volatile("s_nop 1");
      const unsigned o0 = kh * 2 * KB + kh * KB, o1 = kh * 2 * KB + (kh ^ 1) * KB;
      v4u w4q[86];
      unsigned yf;
      auto ldh = [&](auto N) { hf[N.v] = ldg16(rs, l16 + (N.v & 3) * KB, hb4 + (N.v >> 2) * 4 * KB); };
      auto p4_addr = [&](auto I) { return ((I.v >> 5) == 1 ? sB : sA) + ((I.v >> 1) & 15) * 4 * KB + ((I.v & 1) ? o1 : o0); };
      wfirst<PD>(w4q, L, p4_addr);
      wstream<0, 86, 86, PD, GW, 32, 64>(w4q, L, p4_addr,
        [&](auto I, v4u wf) { mma32<I.v == 1>(acc[I.v & 1], wf, hf[I.v >> 1]); },
        [&](auto I) {
          constexpr int i = I.v;
          if constexpr (i == 0 || i == 2) ldh(IC<14 + i / 2>{});
          if constexpr (i == 8 || i == 40) yf = *(volatile LAS unsigned*)(L + L_FLG + 16);
          if constexpr (i == 11) { WAITV(10); mark(16 * layer + P_H2); }
          if constexpr (i == 12) await(yf, 16 * layer + Y_H1);
          if constexpr (i >= 12 && i < 28) ldh(IC<i + 4>{});
          if constexpr (i == 44) await(yf, 16 * layer + Y_H2);
          if constexpr (i >= 44 && i < 55) ldh(IC<i - 12>{});
          if constexpr (i == A_X2) mark(16 * layer + P_X2);
          if constexpr (i == 1) STAMP(12 + 20 * layer);
          if constexpr (i == 13) STAMP(19 + 20 * layer);
          if constexpr (i == 31) STAMP(17 + 20 * layer);
          if constexpr (i == 45) STAMP(15 + 20 * layer);
          if constexpr (i == 63) STAMP(18 + 20 * layer);
        });
      MFMA_DONE();
      SB();
      STAMP(13 + 20 * layer);
#pragma unroll
      for (int q = 0; q < 4; ++q) *(LAS f32x4*)(L + L_EXC + (w * 4 + q) * 1024 + l16) = f32x4{acc[1][4 * q], acc[1][4 * q + 1], acc[1][4 * q + 2], acc[1][4 * q + 3]};
      BAR();
      f32x4 p[4];
#pragma unroll
      for (int q = 0; q < 4; ++q) p[q] = *(LAS f32x4*)(L + L_EXC + ((w ^ 2) * 4 + q) * 1024 + l16);
      mark(16 * layer + P_EX);
      SB();
#pragma unroll
      for (int q = 0; q < 4; ++q) {
        xr[2 * q] = nf(pk(acc[0][4 * q] + p[q][0], acc[0][4 * q + 1] + p[q][1]));
        xr[2 * q + 1] = nf(pk(acc[0][4 * q + 2] + p[q][2], acc[0][4 * q + 3] + p[q][3]));
      }
      if (layer == 0) {
#pragma unroll
        for (int c = 0; c < 2; ++c)
          stg16(v4u{xr[4 * c], xr[4 * c + 1], xr[4 * c + 2], xr[4 * c + 3]}, rs, l16 + c * KB, XA_OFF + X_SZ + (t32b * 32 + 2 * cbk) * KB);
        SB();
        const f32x2 st = block_stats(xr);
        if (h == 0) stg8(v2u{nf(f2u(st.x)), nf(f2u(st.y))}, rs, r * 8, STA_OFF + ST_SZ + (t32b * 16 + cbk) * 256);
      } else {
#pragma unroll
        for (int q = 0; q < 4; ++q) *(v2u*)(out + (size_t)(32 * t32b + r) * D_ + 32 * cbk + 8 * q + 4 * h) = v2u{xr[2 * q], xr[2 * q + 1]};
      }
#pragma unroll
      for (int i = 0; i < 8; ++i) PIN(xr[i]);
      STAMP(14 + 20 * layer);
    }
  }
  STAMP_OUT();
}
}

extern "C" void kernel_launch(void* const* d_in, const int* in_sizes, int n_in, void* d_out, int out_size, void* d_ws, size_t ws_size, hipStream_t stream) {
  const unsigned short* x = (const unsigned short*)d_in[0];
  const unsigned short* wqkv = (const unsigned short*)d_in[1];
  const unsigned short* wo = (const unsigned short*)d_in[2];
  const unsigned short* wg = (const unsigned short*)d_in[4];
  const unsigned short* wu = (const unsigned short*)d_in[5];
  const unsigned short* wd = (const unsigned short*)d_in[6];
  unsigned char* ws = (unsigned char*)d_ws;
  k_prep<<<256, 512, 0, stream>>>(x, wqkv, wo, wg, wu, wd, ws);
  (void)hipFuncSetAttribute((const void*)k_main, hipFuncAttributeMaxDynamicSharedMemorySize, L_END);
  k_main<<<NWG, 512, L_END, stream>>>(x, (unsigned short*)d_out, ws);
}
```

```cpp
#include <hip/hip_runtime.h>
#include <hip/hip_bf16.h>
#include <stdint.h>

#define GAS __attribute__((address_space(1)))
#define LAS __attribute__((address_space(3)))
#define DI __device__ __forceinline__
typedef unsigned v4u __attribute__((ext_vector_type(4)));
typedef unsigned v2u __attribute__((ext_vector_type(2)));
typedef float f32x2 __attribute__((ext_vector_type(2)));
typedef float f32x4 __attribute__((ext_vector_type(4)));
typedef float f32x16 __attribute__((ext_vector_type(16)));
typedef __bf16 bf16x8 __attribute__((ext_vector_type(8)));
typedef __bf16 bf16x2 __attribute__((ext_vector_type(2)));
typedef short s16x4 __attribute__((ext_vector_type(4)));
typedef GAS unsigned gu32;
#define RLX_AGENT __ATOMIC_RELAXED, __HIP_MEMORY_SCOPE_AGENT

#define STAMPS 0

#define A_O 1
#define D_O 0
#define A_X1 26
#define NBV 9
#define PDV 7
#define STSLEEP 6
#define TGV 54
#define WAUX 16
#define D_W1B 3
#define K0V 9
#define HOLD 99
#define STFIRST 0
#define R4EARLY 1
#define THR 0
#define EXP 0
#define SYPRIO 3
namespace {
constexpr int S_ = 2048, D_ = 512, F_ = 1376;
constexpr int NWG = 256;
constexpr unsigned KB = 1024;
constexpr unsigned CTRL_OFF = 0;
constexpr unsigned CTRL_BYTES = 256 * 16 * 128;
constexpr unsigned TMO_OFF = CTRL_OFF + CTRL_BYTES;
constexpr unsigned EP_OFF = TMO_OFF + 128;
constexpr unsigned HS_OFF = 832 * 1024;
constexpr unsigned CP_OFF = TMO_OFF + 4096;
constexpr unsigned WJ_OFF = 1024 * KB;
constexpr unsigned ACT_OFF = WJ_OFF + 16 * 288 * KB;
constexpr unsigned XA_O = 0, STA_O = 128 * KB, ACT_L = 144 * KB;
constexpr unsigned O32_O = 288 * KB, X32_O = 416 * KB, STB_O = 544 * KB;
constexpr unsigned H32_O = 560 * KB;
constexpr unsigned ACT_G = 909 * KB;
constexpr unsigned STAMP_OFF = ACT_OFF + 16 * ACT_G;
constexpr unsigned WS_END = STAMP_OFF + 256 * 8 * 64 * 4;
constexpr int L_SLOT = 0;
constexpr int L_EXC = 128 * 1024;
constexpr int L_KV = L_EXC + 16 * 1024;
constexpr int L_C1 = L_KV + 5 * 2048;
constexpr int L_C3 = L_C1 + 96 * 4;
constexpr int L_ST = L_C3 + 192 * 4;
constexpr int L_FLG = L_ST + 2048;
constexpr int L_END = L_FLG + 32;
constexpr int mapX(int f) { return f < 48 ? f : f + 48; }
constexpr int mapY(int f) { return f < 48 ? f + 48 : f + 64; }
constexpr int mapZ(int f) { return f < 28 ? f + 68 : f + 84; }
static_assert(L_END <= 160 * 1024, "LDS");
constexpr unsigned SPIN_CAP = 1u << 17, RETRY_CAP = 1u << 13;

DI v4u ldg16(__amdgpu_buffer_rsrc_t rs, unsigned voff, unsigned soff) { return __builtin_amdgcn_raw_buffer_load_b128(rs, voff, soff, 16); }
DI v2u ldg8(__amdgpu_buffer_rsrc_t rs, unsigned voff, unsigned soff) { return __builtin_amdgcn_raw_buffer_load_b64(rs, voff, soff, 16); }
DI void stg16(v4u v, __amdgpu_buffer_rsrc_t rs, unsigned voff, unsigned soff) {
  asm volatile("" : "+v"(v));
  __builtin_amdgcn_raw_buffer_store_b128(v, rs, voff, soff, 16);
  asm volatile("s_nop 1" ::"v"(v));
}
DI void stg8(v2u v, __amdgpu_buffer_rsrc_t rs, unsigned voff, unsigned soff) { __builtin_amdgcn_raw_buffer_store_b64(v, rs, voff, soff, 16); }
DI f32x4 mfma16(v4u a, v4u b, f32x4 c) { return __builtin_amdgcn_mfma_f32_16x16x32_bf16(__builtin_bit_cast(bf16x8, a), __builtin_bit_cast(bf16x8, b), c, 0, 0, 0); }
DI f32x4 mfma16k16(v2u a, v2u b, f32x4 c) { return __builtin_amdgcn_mfma_f32_16x16x16bf16_1k(__builtin_bit_cast(s16x4, a), __builtin_bit_cast(s16x4, b), c, 0, 0, 0); }
DI f32x16 mfma32(v4u a, v4u b, f32x16 c) { return __builtin_amdgcn_mfma_f32_32x32x16_bf16(__builtin_bit_cast(bf16x8, a), __builtin_bit_cast(bf16x8, b), c, 0, 0, 0); }
template <bool FIRST> DI void mma16(f32x4& c, v4u a, v4u b) {
  if constexpr (FIRST) asm volatile("v_mfma_f32_16x16x32_bf16 %0, %1, %2, 0" : "=&v"(c) : "v"(a), "v"(b));
  else asm volatile("v_mfma_f32_16x16x32_bf16 %0, %1, %2, %0" : "+v"(c) : "v"(a), "v"(b));
}
template <bool FIRST> DI void mma32(f32x16& c, v4u a, v4u b) {
  if constexpr (FIRST) asm volatile("v_mfma_f32_32x32x16_bf16 %0, %1, %2, 0" : "=&v"(c) : "v"(a), "v"(b));
  else asm volatile("v_mfma_f32_32x32x16_bf16 %0, %1, %2, %0" : "+v"(c) : "v"(a), "v"(b));
}
DI void mma32c(f32x16& d, v4u a, v4u b, const f32x16& c) { asm volatile("v_mfma_f32_32x32x16_bf16 %0, %1, %2, %3" : "=&v"(d) : "v"(a), "v"(b), "v"(c)); }
#define MFMA_DONE() asm volatile("s_nop 11" ::: "memory")
DI unsigned pk(float lo, float hi) { f32x2 v = {lo, hi}; return __builtin_bit_cast(unsigned, __builtin_convertvector(v, bf16x2)); }
DI float blo(unsigned d) { return __builtin_bit_cast(float, d << 16); }
DI float bhi(unsigned d) { return __builtin_bit_cast(float, d & 0xffff0000u); }
DI float u2f(unsigned u) { return __builtin_bit_cast(float, u); }
DI unsigned f2u(float f) { return __builtin_bit_cast(unsigned, f); }
DI float xsum32(float x) { auto r = __builtin_amdgcn_permlane32_swap(f2u(x), f2u(x), false, false); float d; asm("v_add_f32 %0, %1, %2" : "=v"(d) : "v"(u2f(r[0])), "v"(u2f(r[1]))); return d; }
DI float xsum16(float x) { auto r = __builtin_amdgcn_permlane16_swap(f2u(x), f2u(x), false, false); float d; asm("v_add_f32 %0, %1, %2" : "=v"(d) : "v"(u2f(r[0])), "v"(u2f(r[1]))); return d; }
DI float vmax(float a, float b) { float d; asm("v_max_f32 %0, %1, %2" : "=v"(d) : "v"(a), "v"(b)); return d; }
DI float sadd(float a, float b) { float d; asm("v_add_f32 %0, %1, %2" : "=v"(d) : "v"(a), "v"(b)); return d; }
DI float smul(float a, float b) { float d; asm("v_mul_f32 %0, %1, %2" : "=v"(d) : "v"(a), "v"(b)); return d; }
DI float sfma(float a, float b, float c) { float d; asm("v_fma_f32 %0, %1, %2, %3" : "=v"(d) : "v"(a), "v"(b), "v"(c)); return d; }
DI float vmax3(float a, float b, float c) { float d; asm("v_max3_f32 %0, %1, %2, %3" : "=v"(d) : "v"(a), "v"(b), "v"(c)); return d; }
DI float pick(bool p, float a, float b) { asm("" : "+v"(a)); return p ? b : a; }
DI float xmax32(float x) { auto r = __builtin_amdgcn_permlane32_swap(f2u(x), f2u(x), false, false); return vmax(u2f(r[0]), u2f(r[1])); }
DI float xmax16(float x) { auto r = __builtin_amdgcn_permlane16_swap(f2u(x), f2u(x), false, false); return vmax(u2f(r[0]), u2f(r[1])); }
DI float dot2(unsigned a, unsigned b, float c) { return __builtin_amdgcn_fdot2_f32_bf16(__builtin_bit_cast(bf16x2, a), __builtin_bit_cast(bf16x2, b), c, false); }
#define PIN(x) asm volatile("" : "+v"(x))
#define BAR() asm volatile("s_waitcnt lgkmcnt(0)\n\ts_barrier" ::: "memory")
#define DRAIN() asm volatile("s_waitcnt vmcnt(0)" ::: "memory")
#define WAITL(n) __builtin_amdgcn_s_waitcnt(0xC07F | ((n) << 8))
#define WAITV(n) asm volatile("s_waitcnt vmcnt(" #n ")" ::: "memory")

DI int cnt(int wg, int hid) { return wg * 64 + (hid & 1) * 32 + (hid >> 1); }
DI unsigned act(int g, int layer) { return ACT_OFF + g * ACT_G + layer * ACT_L; }
DI int unit_start(int j) { return j < 6 ? 6 * j : 36 + 5 * (j - 6); }
DI int hpos(int j, int lu) {
  if (lu < 4) return (lu & 1) * 43 + 16 * (lu >> 1) + j;
  if (lu == 4) return j < 11 ? 32 + j : 43 + 32 + (j - 11);
  return 43 + 37 + j;
}
DI int hunit(int kh, int s) {
  if (s < 32) return unit_start(s & 15) + 2 * (s >> 4) + kh;
  if (kh == 0) return unit_start(s - 32) + 4;
  return s < 37 ? unit_start(s - 32 + 11) + 4 : unit_start(s - 37) + 5;
}

DI f32x2 block_stats(const unsigned (&p)[8]) {
  float s0 = 0.f, s1 = 0.f, q0 = 0.f, q1 = 0.f;
#pragma unroll
  for (int i = 0; i < 8; i += 2) { s0 = dot2(p[i], 0x3f803f80u, s0); q0 = dot2(p[i], p[i], q0); s1 = dot2(p[i + 1], 0x3f803f80u, s1); q1 = dot2(p[i + 1], p[i + 1], q1); }
  return f32x2{xsum32(s0 + s1), xsum32(q0 + q1)};
}
DI float ln_var(float s, float q, float& mean) {
  mean = s * (1.f / 512.f);
  PIN(mean);
  return fmaxf(fmaf(-mean, mean, q * (1.f / 512.f)), 0.f) + 1e-5f;
}

constexpr unsigned FILL = 0xffffffffu;
DI unsigned nf(unsigned d) { return min(d, 0xfffffffeu); }
DI unsigned chk(unsigned m, v4u a) { return max(max(max(max(m, a.x), a.y), a.z), a.w); }

enum { H_X = 0, H_O = 1, H_X1 = 2, H_H0 = 3, H_H1 = 4, H_H2 = 5, H_W = 12 };
enum { P_0 = 1,
       P_A,
       P_H,
       P_K,
       P_O, P_X1,
       P_E2,
       P_30, P_31,
       P_H0,
       P_U4, P_32,
       P_H1, P_H2,
       P_40, P_41,
       P_E4,
       P_X2 };
enum { Y_X = 1, Y_ST, Y_W, Y_KV, Y_O, Y_X1, Y_S3, Y_H0, Y_H1, Y_H2 };
enum { Z_T = 1, Z_1A, Z_1B, Z_F, Z_2, Z_30, Z_31, Z_32, Z_40, Z_41, Z_42 };
constexpr unsigned LSPIN = 1u << 20;

#define STAMP(idx) do {} while (0)
#define STAMP_RT(idx) do {} while (0)
#define STAMP_OUT() do {} while (0)

template <int I> struct IC { static constexpr int v = I; };
template <int B, int E, class Fn> DI void sfor(Fn&& f) { if constexpr (B < E) { f(IC<B>{}); sfor<B + 1, E>(f); } }
#define SB() __builtin_amdgcn_sched_barrier(0)
#define FENCE() asm volatile("" ::: "memory")
DI int sopaque(int v) { asm volatile("" : "+s"(v)); return v; }
DI int opaque(int v) { asm volatile("" : "+v"(v)); return v; }
DI unsigned rfl(unsigned v) { return __builtin_amdgcn_readfirstlane(v); }
DI unsigned lds_word(LAS unsigned char* L, unsigned a) { return *(volatile LAS unsigned*)(L + a); }
DI v4u lds_words(LAS unsigned char* L, unsigned a) { return *(volatile LAS v4u*)(L + a); }

DI void dma_groups(__amdgpu_buffer_rsrc_t rs, unsigned src, LAS unsigned char* dst, int n, unsigned l16) {
#pragma unroll 1
  for (int i = 0; i < n; ++i) {
    __builtin_amdgcn_raw_ptr_buffer_load_lds(rs, (LAS void*)dst, 16, l16, src, 0, WAUX);
    __builtin_amdgcn_raw_ptr_buffer_load_lds(rs, (LAS void*)dst, 16, l16, src, 1024, WAUX);
    __builtin_amdgcn_raw_ptr_buffer_load_lds(rs, (LAS void*)dst, 16, l16, src, 2048, WAUX);
    __builtin_amdgcn_raw_ptr_buffer_load_lds(rs, (LAS void*)dst, 16, l16, src, 3072, WAUX);
    dst += 12288; src += 12288;
  }
}
template <int B, int E, int NF, int PD, int GW, int RPF, class FA, class FM, class FE> DI void wstream(v4u (&w)[NF], FA&& frag, FM&& mma, FE&& extra) {
  sfor<B, E>([&](auto I) {
    constexpr int i = I.v, n = i + PD;
    if constexpr (i % GW == 0 && n <= NF) WAITL(RPF * (PD - GW));
    mma(I, w[i]);
    SB();
    if constexpr (n < NF) w[n] = frag(IC<n>{});
    extra(I);
    SB();
  });
}
constexpr int p1_sl(int s) { return s < 63 ? s / 3 : 21 + (s - 63) % 11; }
constexpr int p1_mt(int s) { return s < 63 ? s % 3 : ((s - 63) / 11 + 1) % 3; }
constexpr bool p1_two(int s) { return (p1_sl(s) & 15) < 4 && p1_mt(s) != 0; }
struct P1Tab {
  int gap[97], items[33], slot[40], at[112];
  constexpr P1Tab(int nb) : gap(), items(), slot(), at() {
    for (int s = 0; s < 96; ++s) gap[s + 1] = gap[s] + (p1_two(s) ? 2 : 1);
    for (int i = 0; i < 32; ++i) { items[i + 1] = items[i] + (((i & 15) < 4) ? 2 : 1); for (int n = items[i]; n < items[i + 1]; ++n) slot[n] = i; }
    for (int g = 0; g < 112; ++g) at[g] = -1;
    int g = -2;
    for (int n = nb; n < 40; ++n) {
      const int i = slot[n], e = (i < 21 ? gap[3 * i] : gap[63 + i - 21]) - (n < 16 ? 30 : 33);
      g += 2; if (g < e) g = e;
      at[g] = n;
    }
  }
};
constexpr P1Tab P1T(NBV);
constexpr int p1_gap(int s) { return P1T.gap[s]; }
constexpr int EP_N = 87;
constexpr int ep_op(int k) {
  if (k == 86) return 43;
  const int o = k % 43;
  return o < 8 ? o : o == 8 ? 42 : o - 1;
}
constexpr int ep_weight(int k) { const int o = ep_op(k); return o == 42 ? 5 : o == 43 ? 5 : ((o >> 2) == 6 || (o >> 2) == 8) ? 2 : 1; }
struct EpTab {
  int done[192];
  constexpr EpTab() : done() {
    int total = 0; for (int k = 0; k < EP_N; ++k) total += ep_weight(k);
    int n = 0, c = 0;
    for (int step = 67; step < 191; ++step) {
      const int budget = (5 * total * (step - 66)) / 125;
      while (n < 5 * EP_N && c + ep_weight(n % EP_N) <= budget) { c += ep_weight(n % EP_N); ++n; }
      done[step] = n;
    }
    done[191] = 5 * EP_N;
  }
};
constexpr EpTab EPT;
constexpr int ep_done(int step) { return EPT.done[step]; }

DI void stage_rows(const unsigned short* src, LAS unsigned char* dst) { __builtin_amdgcn_global_load_lds((const GAS void*)src, (LAS void*)dst, 16, 0, 0); }
DI int tr_lane(int lane, bool perm) { return ((perm ? 4 : 8) * (lane >> 5) + ((lane & 15) >> 2)) * 64 + ((lane >> 4) & 1) * 32 + (lane & 3) * 8; }
DI int tr_lane2(int lane, bool perm, int nt) { const int q = (lane & 15) >> 2; return ((perm ? 4 : 8) * (lane >> 5) + q) * 128 + ((nt ^ (q >> 1)) * 2 + ((lane >> 4) & 1)) * 32 + (lane & 3) * 8; }
DI v4u read_tr2(LAS unsigned char* p, int d) {
  const v2u l2 = __builtin_bit_cast(v2u, __builtin_amdgcn_ds_read_tr16_b64_v4i16((LAS s16x4*)p)), h2 = __builtin_bit_cast(v2u, __builtin_amdgcn_ds_read_tr16_b64_v4i16((LAS s16x4*)(p + d)));
  return v4u{l2.x, l2.y, h2.x, h2.y};
}
DI v4u read_tr(LAS unsigned char* img, int lane, bool perm) { return read_tr2(img + tr_lane(lane, perm), perm ? 512 : 256); }

__global__ void __launch_bounds__(512, 2) k_main(const unsigned short* __restrict__ x, const unsigned short* __restrict__ wqkv, const unsigned short* __restrict__ wo,
                                                 const unsigned short* __restrict__ wg, const unsigned short* __restrict__ wu, const unsigned short* __restrict__ wd,
                                                 unsigned short* __restrict__ out, unsigned char* __restrict__ ws) {
  extern __shared__ __attribute__((aligned(16))) unsigned char smem_[];
  LAS unsigned char* L = (LAS unsigned char*)smem_;
  const int b = blockIdx.x;
  const int g = ((b & 7) >> 1) * 4 + (b >> 6), j = (b & 1) * 8 + ((b >> 3) & 7);
  const int wv = __builtin_amdgcn_readfirstlane(threadIdx.x >> 6), lane = threadIdx.x & 63;
  const __amdgpu_buffer_rsrc_t rs = __builtin_amdgcn_make_buffer_rsrc(ws, 0, WS_END, 0x00020000);
  const int rh = j >> 3, cb = j & 7;
  const bool six = j < 6;
  auto progress = [&](unsigned want) {
    for (unsigned spins = 0; spins < LSPIN; ++spins) {
      const v4u f = lds_words(L, L_FLG);
      if (rfl(min(min(f.x, f.y), min(f.z, f.w))) >= want) break;
      __builtin_amdgcn_s_sleep(1);
    }
    FENCE();
  };
  auto mark = [&](unsigned v) { FENCE(); *(volatile LAS unsigned*)(L + L_FLG + wv * 4) = v; };

  auto tile_in = [&](int t, LAS unsigned char* img) {
    const int row = lane >> 2, p = lane & 3;
    const unsigned short* src = t < 3 ? wqkv + (size_t)(32 * g + row) * 1536 + t * 512 + 32 * j + 8 * p
                                      : (p < 2 ? wg : wu) + (size_t)(32 * g + row) * F_ + (unit_start(j) + ((t < 8 || six) ? t - 3 : 0)) * 16 + 8 * (p & 1);
    stage_rows(src, img);
    stage_rows(src + 16 * (t < 3 ? 1536 : F_), img + 1024);
  };
  auto tile_sum = [&](int t, v4u f0, v4u f1) {
    float sm = 0.f;
#pragma unroll
    for (int i = 0; i < 4; ++i) { sm = dot2(f0[i], 0x3f803f80u, sm); sm = dot2(f1[i], 0x3f803f80u, sm); }
    sm = xsum32(sm);
    __builtin_amdgcn_raw_buffer_store_b32(f2u(sm), rs, lane < 32 ? lane * 4 : 0x40000000u, CP_OFF + ((j * 16 + g) * 288 + t * 32) * 4, 16);
  };
  auto tile_put = [&](int t, v4u s0, v4u s1) {
    const int lu = t - 3;
    const unsigned d0 = WJ_OFF + (j * 288 + (t < 3 ? 6 * g + t : 96 + (lu < 2 ? 4 * g + lu : lu * 32 + 2 * g))) * KB;
    stg16(s0, rs, lane * 16, d0);
    stg16(s1, rs, lane * 16, d0 + (t < 3 ? 3 : lu < 2 ? 2 : 1) * KB);
  };

  auto colsums = [&](auto T0, auto T1, auto&& between) {
    v4u p[T1.v - T0.v][2];
    sfor<T0.v, T1.v>([&](auto T) { sfor<0, 2>([&](auto I) { p[T.v - T0.v][I.v] = ldg16(rs, (lane >> 3) * 1152 + (lane & 7) * 16, CP_OFF + (j * 16 + 8 * I.v) * 1152 + T.v * 128); }); });
    SB();
    between();
    sfor<T0.v, T1.v>([&](auto T) {
      float c[4];
      sfor<0, 4>([&](auto E) { c[E.v] = sadd(u2f(p[T.v - T0.v][0][E.v]), u2f(p[T.v - T0.v][1][E.v])); });
      asm volatile("s_nop 1" : "+v"(c[0]), "+v"(c[1]), "+v"(c[2]), "+v"(c[3]));
      auto x = __builtin_amdgcn_permlane32_swap(f2u(c[0]), f2u(c[1]), false, false), y = __builtin_amdgcn_permlane32_swap(f2u(c[2]), f2u(c[3]), false, false);
      float xs = sadd(u2f(x[0]), u2f(x[1])), ys = sadd(u2f(y[0]), u2f(y[1]));
      asm volatile("s_nop 1" : "+v"(xs), "+v"(ys));
      auto z = __builtin_amdgcn_permlane16_swap(f2u(xs), f2u(ys), false, false);
      float zs = sadd(u2f(z[0]), u2f(z[1]));
      zs += u2f(__builtin_amdgcn_update_dpp(0u, f2u(zs), 0x128, 0xf, 0xf, false));
      if (!(lane & 8)) *(LAS float*)(L + L_C1 + (T.v * 32 + (lane & 7) * 4 + 2 * ((lane >> 4) & 1) + (lane >> 5)) * 4) = zs;
      SB();
    });
  };

  auto fill = [&](int li, int k0, int k1) {
    const v4u ff = {FILL, FILL, FILL, FILL};
#pragma unroll
    for (int k = k0; k < k1; ++k) {
      const int i = 3 * k + li;
      const unsigned so = act(g, 0) + (i < 8 ? O32_O + ((i >> 1) * 32 + 2 * j + (i & 1)) * KB : i < 16 ? X32_O + ((2 * rh + ((i >> 2) & 1)) * 32 + 4 * cb + (i & 3)) * KB : STB_O + (2 * rh * 16 + 2 * cb) * 256);
      stg16(ff, rs, i < 16 ? lane * 16 : i == 16 ? (lane >> 5) * 4096 + (lane & 31) * 16 : 0x40000000u, so);
    }
  };

  if (wv >= 5) {
    const int li = wv - 5;
    const unsigned w1 = WJ_OFF + j * 288 * KB, w3 = w1 + 96 * KB;
    const unsigned l16 = lane * 16, lo = opaque(L_SLOT + li * KB + l16), hi = opaque(L_SLOT + 65536 + li * KB + l16);
    auto dma = [&](unsigned src, int dst, int n) { dma_groups(rs, src + 4 * li * KB, L + L_SLOT + (dst + 4 * li) * KB, (n / 4 + 2 - li) / 3, l16); };
    auto at = [&](int kb) { return kb < 64 ? L + lo + kb * KB : L + hi + (kb - 64) * KB; };
    const __amdgpu_buffer_rsrc_t ro = __builtin_amdgcn_make_buffer_rsrc((void*)wo, 0, 512 * 512 * 2, 0x00020000), rd = __builtin_amdgcn_make_buffer_rsrc((void*)wd, 0, F_ * 512 * 2, 0x00020000);
    const unsigned vr = (lane >> 3) * 1024 + ((lane & 7) ^ ((lane >> 2) & 4)) * 16 + 128 * cb;
    auto rows32 = [&](bool down, int f0, int dst, int n) {
#pragma unroll 1
      for (int q = li; q < n / 4; q += 3) {
        const int s = f0 / 4 + q;
        const unsigned so = down ? (unit_start(s & 15) + 2 * (s >> 4)) * 16 * KB : s * 32 * KB;
        LAS unsigned char* d = L + L_SLOT + (dst + 4 * q) * KB;
        __builtin_amdgcn_raw_ptr_buffer_load_lds(down ? rd : ro, (LAS void*)d, 16, vr, so, 0, 0);
        __builtin_amdgcn_raw_ptr_buffer_load_lds(down ? rd : ro, (LAS void*)d, 16, vr + 7 * KB, so, 1024, 0);
        __builtin_amdgcn_raw_ptr_buffer_load_lds(down ? rd : ro, (LAS void*)d, 16, vr + 14 * KB, so, 2048, 0);
        __builtin_amdgcn_raw_ptr_buffer_load_lds(down ? rd : ro, (LAS void*)d, 16, vr + 21 * KB, so, 3072, 0);
      }
    };
    v4u r2[16], r3[20], r4[20];
    auto loaders = [&](unsigned z) { for (unsigned spins = 0; spins < LSPIN; ++spins) { const v4u t = lds_words(L, L_FLG + 16); if (rfl(min(min(t.y, t.z), t.w)) >= z) break; __builtin_amdgcn_s_sleep(1); } FENCE(); };
    auto helper = [&](unsigned y) { for (unsigned spins = 0; spins < LSPIN && rfl(lds_word(L, L_FLG + 16)) < y; ++spins) __builtin_amdgcn_s_sleep(1); FENCE(); };
    {
      { const int layer = 0; STAMP(12); }
      BAR();
      LAS unsigned char* img = L + L_EXC + 4 * li * KB;
      tile_in(3 + 2 * li, img); tile_in(4 + 2 * li, img + 2 * KB);
      {
        const unsigned short* p = li == 0 ? wo + (size_t)b * 1024 + lane * 8 : wd + (size_t)b * 2752 + (li - 1) * 1536 + lane * 8;
        v4u t[3];
#pragma unroll
        for (int i = 0; i < 3; ++i) t[i] = *(const v4u*)(p + ((i < 2 || li == 1 || (li == 2 && lane < 24)) ? i * 512 : 0));
        asm volatile("" :: "v"(t[0]), "v"(t[1]), "v"(t[2]));
      }
      DRAIN();
      { const int layer = 0; STAMP(13); }
      v4u tf[4];
#pragma unroll
      for (int i = 0; i < 4; ++i) tf[i] = read_tr(img + i * KB, lane, true);
      progress(P_0);
      __builtin_amdgcn_s_sleep(4);
      tile_put(3 + 2 * li, tf[0], tf[1]); tile_put(4 + 2 * li, tf[2], tf[3]); tile_sum(3 + 2 * li, tf[0], tf[1]); tile_sum(4 + 2 * li, tf[2], tf[3]);
      sfor<0, 20>([&](auto I) { const int f = 112 + 3 * I.v + li; r4[I.v] = __builtin_amdgcn_raw_buffer_load_b128(rd, vr, (hunit((f >> 1) & 1, f >> 2) * 2 + (f & 1)) * 8 * KB, 0); });
      WAITV(20);
      mark(Z_T);
      { const int layer = 0; STAMP(11); }
      helper(Y_X);
      dma(w1, 0, 72);
      WAITV(8); mark(Z_1A);
      { const int layer = 0; STAMP(10); }
      dma(w1 + 72 * KB, 72, 24);
      WAITV(0); mark(Z_1B);
      { const int layer = 0; STAMP(14); }
      fill(li, 0, 3);
      WAITV(0); mark(Z_F);
    }
    sfor<0, 2>([&](auto LY) {
      constexpr int layer = LY.v, P = 32 * layer;
      if constexpr (layer == 1) { WAITV(0); mark(P + Z_1B); }
      STAMP(1);
      if constexpr (layer == 0) { helper(Y_W); auto no = [] {}; if (li == 0) colsums(IC<3>{}, IC<5>{}, no); else if (li == 1) colsums(IC<5>{}, IC<7>{}, no); else colsums(IC<7>{}, IC<9>{}, no); }
      progress(P + P_A);
      rows32(false, 48, 112, 16);
      if constexpr (layer == 0) { sfor<0, 16>([&](auto I) { r2[I.v] = __builtin_amdgcn_raw_buffer_load_b128(ro, vr, (3 * I.v + li) * 8 * KB, 0); }); fill(li, 3, 6); }
      dma(w3, 0, 48);
      if constexpr (layer == 0) dma(w3 + 48 * KB, 96, 16);
      STAMP(2);
      progress(P + P_K);
      sfor<0, 16>([&](auto I) { *(LAS v4u*)at(48 + 3 * I.v) = r2[I.v]; });
      WAITV(16); mark(P + Z_2); STAMP(3);
      WAITV(0); mark(P + Z_30); STAMP(4);
      progress(P + P_E2);
      if constexpr (layer == 0) {
        dma(w3 + 64 * KB, 48, 48); dma(w3 + 112 * KB, 112, 16);
        WAITV(0); mark(Z_31);
        loaders(Z_31);
        sfor<0, 20>([&](auto I) { r3[I.v] = *(LAS v4u*)at(mapY(3 * I.v)); });
      } else {
        dma(w3 + 124 * KB, 124, 4);
        sfor<0, 20>([&](auto I) { *(LAS v4u*)at(mapY(3 * I.v)) = r3[I.v]; });
        WAITV(0); mark(P + Z_31);
      }
      STAMP(5);
      progress(P + P_30);
      dma(w3 + 128 * KB, 0, six ? 48 : 32);
      if (six) dma(w3 + 176 * KB, 96, 16);
      WAITV(0); mark(P + Z_32); STAMP(6);
      progress(P + P_31);
      rows32(true, 0, 48, 48); rows32(true, 48, 112, 16);
      progress(P + P_U4);
      rows32(true, 64, 0, 32);
      WAITV(8); mark(P + Z_40); STAMP(7);
      progress(P + P_32);
      rows32(true, 96, 32, 16);
      sfor<0, 5>([&](auto I) { *(LAS v4u*)at(96 + 3 * I.v) = r4[I.v]; });
      if (li == 0) *(LAS v4u*)at(111) = r4[5];
      WAITV(0); mark(P + Z_41); STAMP(8);
      progress(P + P_40);
      if (li) *(LAS v4u*)at(67) = r4[5];
      sfor<6, 20>([&](auto I) {
        constexpr int f = 3 * I.v - 16;
        if constexpr (f == 26) *(LAS v4u*)(at(94) + (li == 2 ? 16 * KB : 0)) = r4[I.v];
        else *(LAS v4u*)at(mapZ(f)) = r4[I.v];
      });
      mark(P + Z_42); STAMP(9);
      if constexpr (layer == 0) {
        fill(li, 0, 6);
        progress(P_41);
        dma(w1, 0, 48); dma(w1 + 48 * KB, 48, 20); dma(w3 + 48 * KB, 96, 16);
        WAITV(8); mark(32 + Z_1A);
        progress(P_E4);
        dma(w1 + 68 * KB, 68, 28);
      }
    });
    STAMP_OUT();
    return;
  }

  if (wv == 4) {
    __builtin_amdgcn_s_setprio(SYPRIO);
    if (lane < 8) *(volatile LAS unsigned*)(L + L_FLG + lane * 4) = 0u;
    BAR();
    gu32* ctrl = (gu32*)(ws + CTRL_OFF);
    gu32* tmo = (gu32*)(ws + TMO_OFF);
    const unsigned epoch = __hip_atomic_load((gu32*)(ws + EP_OFF), RLX_AGENT);
    unsigned tag;
    const int tb = (g + (lane >> 4)) * 16 + (lane & 15);
    auto add = [&](int hid, bool next) {
      if (lane < 16 || (next && lane < 32)) __hip_atomic_fetch_add(ctrl + cnt(tb, hid), 1u, RLX_AGENT);
    };
    auto poll = [&](int hid, unsigned want) {
      gu32* w = ctrl + cnt(g * 16 + j, hid);
      for (unsigned spins = 0;; ++spins) {
        if (rfl(__hip_atomic_load(w, RLX_AGENT)) >= want) break;
        if (spins > SPIN_CAP) { __hip_atomic_store(tmo, tag, RLX_AGENT); break; }
        if ((spins & 1023u) == 1023u && rfl(__hip_atomic_load(tmo, RLX_AGENT)) == tag) break;
      }
      __builtin_amdgcn_fence(__ATOMIC_ACQUIRE, "wavefront");
    };
    auto watch = [&](int hid, unsigned y, unsigned want) {
      gu32* c0 = ctrl + cnt(g * 16 + j, hid);
      bool seen = false;
      for (unsigned spins = 0; spins < LSPIN; ++spins) {
        if (!seen && rfl(__hip_atomic_load(c0, RLX_AGENT)) >= 16u) { seen = true; __builtin_amdgcn_fence(__ATOMIC_ACQUIRE, "wavefront"); mark(y); }
        const v4u f = lds_words(L, L_FLG);
        if (rfl(min(min(f.x, f.y), min(f.z, f.w))) >= want) break;
        if (seen) __builtin_amdgcn_s_sleep(1);
      }
      FENCE();
      return seen;
    };
    stg16(v4u{0, 0, 0, 0}, rs, lane < 16 ? lane * 16 : 0x40000000u, CTRL_OFF + (g * 16 + j) * 256);
    DRAIN();
    tag = rfl(epoch) + 1u;
    { const int layer = 0; STAMP(17); }
    progress(P_0);
    {
      const int q = lane >> 4, m = lane & 15;
      const int cw = q == 0 ? g * 16 + m : q == 1 ? (g + 1) * 16 + m : m * 16 + j;
      const int slot = q < 2 ? 16 * q + j : 16 * q + g;
      { const int layer = 0; STAMP(18); }
      stg8(v2u{tag, ~tag}, rs, ((q == 1 && (rh == 0 || g == 15)) || q == 3) ? 0x40000000u : cw * 512 + slot * 8, HS_OFF);
    }
    auto shake = [&](bool need) {
      for (unsigned spins = 0;; ++spins) {
        FENCE();
        const v2u v = ldg8(rs, need ? lane * 8 : 0x40000000u, HS_OFF + (g * 16 + j) * 512);
        if (__builtin_amdgcn_ballot_w64(need && (v.x != tag || v.y != ~tag)) == 0) break;
        if (spins > SPIN_CAP) { __hip_atomic_store(tmo, tag, RLX_AGENT); break; }
      }
      FENCE();
    };
    shake(lane < 16 || (lane >= 24 && lane < 48 && (g > 0 || lane >= 32)));
    mark(Y_X);
    { const int layer = 0; STAMP(19); }
    for (unsigned spins = 0; spins < LSPIN; ++spins) { const v4u t = lds_words(L, L_FLG + 16); if (rfl(min(min(t.y, t.z), t.w)) >= Z_T) break; }
    FENCE();
    if (lane < 16) __hip_atomic_fetch_add(ctrl + cnt(lane * 16 + j, H_W), 1u, RLX_AGENT);
    const int tok = lane & 15, gq = lane >> 4, h = lane >> 5, rtok = 2 * tok + (gq & 1);
    auto row_sums = [&](auto NT, unsigned base, unsigned extra, f32x2 (&sq)[5], unsigned y) {
      for (unsigned tries = 0;; ++tries) {
        FENCE();
        v4u p[NT.v][4];
        sfor<0, NT.v>([&](auto T) { sfor<0, 4>([&](auto C) { p[T.v][C.v] = ldg16(rs, lane * 16 + C.v * 1024, T.v < 4 ? base + T.v * 4096 : extra); }); });
        SB();
        if (y) mark(y);
        bool odd = false;
        sfor<0, NT.v>([&](auto T) {
          float a[4];
          sfor<0, 4>([&](auto E) { a[E.v] = sadd(sadd(u2f(p[T.v][0][E.v]), u2f(p[T.v][1][E.v])), sadd(u2f(p[T.v][2][E.v]), u2f(p[T.v][3][E.v]))); });
          asm volatile("s_nop 1" : "+v"(a[0]), "+v"(a[1]), "+v"(a[2]), "+v"(a[3]));
          auto x = __builtin_amdgcn_permlane16_swap(f2u(a[0]), f2u(a[2]), false, false), y = __builtin_amdgcn_permlane16_swap(f2u(a[1]), f2u(a[3]), false, false);
          float xs = sadd(u2f(x[0]), u2f(x[1])), ys = sadd(u2f(y[0]), u2f(y[1]));
          asm volatile("s_nop 1" : "+v"(xs), "+v"(ys));
          auto z = __builtin_amdgcn_permlane32_swap(f2u(xs), f2u(ys), false, false);
          const float tot = sadd(u2f(z[0]), u2f(z[1]));
          odd |= tot != tot;
          auto b2 = __builtin_amdgcn_permlane32_swap(f2u(tot), f2u(tot), false, false);
          sq[T.v] = f32x2{u2f(b2[0]), u2f(b2[1])};
          SB();
        });
        if (__builtin_amdgcn_ballot_w64(odd) == 0) break;
        unsigned m = 0;
        sfor<0, NT.v>([&](auto T) { sfor<0, 4>([&](auto C) { m = chk(m, p[T.v][C.v]); }); });
        if (__builtin_amdgcn_ballot_w64(m == FILL) == 0) break;
        if (tries > RETRY_CAP) { __hip_atomic_store(tmo, tag, RLX_AGENT); break; }
      }
    };
#pragma unroll 1
    for (int layer = 0; layer < 2; ++layer) {
      const unsigned P = 32 * layer;
      if (layer) { poll(6 + H_X, g ? 24u : 16u); if (!STFIRST) { mark(P + Y_X); __builtin_amdgcn_s_sleep(STSLEEP); } }
      STAMP(1);
      {
        f32x2 sq[5];
        auto sums = [&] { row_sums(IC<5>{}, act(g, layer) + STA_O, act(g ? g - 1 : 0, layer) + STA_O + 3 * 4096, sq, P + Y_X); };
        if (layer == 0) colsums(IC<0>{}, IC<3>{}, sums); else sums();
#pragma unroll
        for (int t = 0; t < 5; ++t) {
          float mean;
          float rstd = __builtin_amdgcn_rsqf(ln_var(sq[t].x, sq[t].y, mean));
          PIN(rstd);
          *(LAS float*)(L + L_ST + h * 640 + (t * 32 + rtok) * 4) = h ? -mean * rstd : rstd;
          SB();
        }
        if (layer == 0) STAMP(21);
        mark(P + Y_ST);
        STAMP(2);
      }
      if (layer == 0) {
        poll(H_W, 16u);
        mark(Y_W);
        STAMP(20);
      }
      {
        const float rstd = *(LAS float*)(L + L_ST + (144 + tok) * 4), mr = *(LAS float*)(L + L_ST + 640 + (144 + tok) * 4);
        const f32x4 r4 = *(LAS f32x4*)(L + L_ST + (144 + 4 * gq) * 4), m4 = *(LAS f32x4*)(L + L_ST + 640 + (144 + 4 * gq) * 4);
        float tk[2][4], tv[2][4];
#pragma unroll
        for (int e = 0; e < 2; ++e) {
          const f32x4 ck = *(LAS f32x4*)(L + L_C1 + (32 + 16 * e + 8 * (gq & 1) + 4 * (gq >> 1)) * 4);
          const float cv = *(LAS float*)(L + L_C1 + (64 + 16 * e + tok) * 4);
#pragma unroll
          for (int i = 0; i < 4; ++i) { tk[e][i] = smul(ck[i], mr); tv[e][i] = smul(m4[i], cv); PIN(tk[e][i]); PIN(tv[e][i]); }
        }
        progress(P + P_H);
        STAMP(3);
        f32x4 kp[2][4], vp[2][4];
#pragma unroll
        for (int e = 0; e < 2; ++e)
#pragma unroll
          for (int w = 0; w < 4; ++w) {
            kp[e][w] = *(LAS f32x4*)(L + L_EXC + w * 4096 + (gq & 1) * 1024 + (tok + 16 * e + 32 * (gq >> 1)) * 16);
            vp[e][w] = *(LAS f32x4*)(L + L_EXC + w * 4096 + 2048 + (gq >> 1) * 1024 + (16 * e + tok + 32 * (gq & 1)) * 16);
          }
        SB();
#pragma unroll
        for (int e = 0; e < 2; ++e) {
          f32x4 k4, v4;
#pragma unroll
          for (int i = 0; i < 4; ++i) {
            k4[i] = sfma(sadd(sadd(kp[e][0][i], kp[e][1][i]), sadd(kp[e][2][i], kp[e][3][i])), rstd, tk[e][i]);
            v4[i] = sfma(sadd(sadd(vp[e][0][i], vp[e][1][i]), sadd(vp[e][2][i], vp[e][3][i])), r4[i], tv[e][i]);
          }
          v4u o = {pk(k4[0], k4[1]), pk(k4[2], k4[3]), pk(v4[0], v4[1]), pk(v4[2], v4[3])};
          if (!g) o = v4u{0, 0, 0, 0};
          *(LAS v4u*)(L + L_KV + e * 1024 + lane * 16) = o;
        }
      }
      mark(P + Y_KV);
      STAMP(4);
      progress(P + P_O);
      if (D_O) __builtin_amdgcn_s_sleep(D_O);
      STAMP(5);
      add(6 * layer + H_O, false);
      poll(6 * layer + H_O, 16u);
      mark(P + Y_O);
      STAMP(6);
      progress(P + P_X1);
      STAMP(7);
      add(6 * layer + H_X1, false);
      poll(6 * layer + H_X1, 16u);
      if (!STFIRST) { mark(P + Y_X1); __builtin_amdgcn_s_sleep(STSLEEP); }
      STAMP(8);
      {
        f32x2 sq[5];
        row_sums(IC<4>{}, act(g, 0) + STB_O, 0, sq, P + Y_X1);
#pragma unroll
        for (int t = 0; t < 4; ++t) {
          float mean;
          float vv = ln_var(sq[t].x, sq[t].y, mean), rstd = __builtin_amdgcn_rsqf(vv);
          PIN(rstd);
          constexpr float c2 = -2.f * 0.7978845608028654f * 1.4426950408889634f, c1 = c2 * 0.044715f;
          float k1 = c1 * rstd; PIN(k1);
          float k2 = c2 * rstd; PIN(k2);
          float r2 = rstd * rstd; PIN(r2);
          k1 *= r2; PIN(k1);
          if (h == 0) *(LAS f32x4*)(L + L_ST + (t * 32 + rtok) * 16) = f32x4{-mean, k1, k2, vv};
          SB();
        }
      }
      mark(P + Y_S3);
      STAMP(9);
      progress(P + P_H0);
      add(6 * layer + H_H0, false);
      STAMP(10);
      const bool s0 = watch(6 * layer + H_H0, P + Y_H0, P + P_H1);
      add(6 * layer + H_H1, false);
      STAMP(11);
      if (!s0) { poll(6 * layer + H_H0, 16u); mark(P + Y_H0); }
      STAMP(12);
      const bool s1 = watch(6 * layer + H_H1, P + Y_H1, P + P_H2);
      add(6 * layer + H_H2, false);
      STAMP(13);
      if (!s1) { poll(6 * layer + H_H1, 16u); mark(P + Y_H1); }
      STAMP(14);
      poll(6 * layer + H_H2, 16u);
      mark(P + Y_H2);
      STAMP(15);
      if (layer == 0) {
        progress(P_X2);
        STAMP(16);
        add(6 + H_X, rh == 1 && g < 15);
      }
    }
    if (g == 0 && j == 0) __hip_atomic_store((gu32*)(ws + EP_OFF), tag, RLX_AGENT);
    STAMP_OUT();
    return;
  }

#define PHASE_SCALARS() const int w = sopaque(wv), g = sopaque(g0), j = sopaque(j0), rh = j >> 3, cb = j & 7, tt = w & 1, kh = w >> 1, t32a = 4 * g + w, tb = 2 * rh + tt, t32b = 4 * g + tb, cbk = 2 * cb + kh; \
  const bool six = j < 6; (void)t32a; (void)t32b; (void)cbk; (void)six
#define PHASE_LANE() PHASE_SCALARS(); const int lane = opaque(lane0), tok = lane & 15, gq = lane >> 4, r = lane & 31, h = lane >> 5; const unsigned l16 = lane * 16; (void)tok; (void)gq; (void)r; (void)h
  const int g0 = g, j0 = j, lane0 = lane;
  unsigned xr[8];
  auto publish = [&](unsigned xo, unsigned so, unsigned l16, int r, int h) {
#pragma unroll
    for (int c = 0; c < 2; ++c) stg16(v4u{xr[4 * c], xr[4 * c + 1], xr[4 * c + 2], xr[4 * c + 3]}, rs, l16 + c * KB, xo);
    SB();
    const f32x2 st = block_stats(xr);
    stg8(v2u{nf(f2u(st.x)), nf(f2u(st.y))}, rs, h ? 0x40000000u : r * 8, so);
  };
  {
    PHASE_SCALARS();
    LAS unsigned char* img = L + L_KV + 2 * w * KB;
    v2u xv[4];
#pragma unroll
    for (int q = 0; q < 4; ++q) xv[q] = *(const v2u*)(x + (size_t)(32 * t32b + (lane & 31)) * D_ + 32 * cbk + 8 * q + 4 * (lane >> 5));
    if (w < 3) tile_in(w, img);
    { const int layer = 0; STAMP(28); }
    SB();
    BAR();
    SB();
    { const int layer = 0; STAMP(29); }
#pragma unroll
    for (int q = 0; q < 4; ++q) { xr[2 * q] = nf(xv[q].x); xr[2 * q + 1] = nf(xv[q].y); }
    DRAIN();
    { const int layer = 0; STAMP(26); }
    publish(act(g, 0) + XA_O + (tb * 32 + 2 * cbk) * KB, act(g, 0) + STA_O + (tb * 16 + cbk) * 256, lane * 16, lane & 31, lane >> 5);
    if (w < 3) { const v4u c0 = read_tr(img, lane, true), c1 = read_tr(img + KB, lane, true); tile_put(w, c0, c1); tile_sum(w, c0, c1); }
    DRAIN();
    mark(P_0);
    { const int layer = 0; STAMP(27); }
  }
  unsigned redo = 0;
  auto again = [&](float probe, unsigned base, int n, unsigned& tries) {
    if (__builtin_expect(__builtin_amdgcn_ballot_w64(probe != probe) == 0 || tries > 3, 1)) return false;
    ++redo;
    if (++tries <= 3) return true;
    for (unsigned spins = 0;; ++spins) {
      FENCE();
      unsigned m = 0;
#pragma unroll 1
      for (int i = 0; i < n; i += 4) m = chk(chk(chk(chk(m, ldg16(rs, lane0 * 16, base + i * KB)), ldg16(rs, lane0 * 16 + KB, base + i * KB)), ldg16(rs, lane0 * 16 + 2 * KB, base + i * KB)), ldg16(rs, lane0 * 16 + 3 * KB, base + i * KB));
      if (__builtin_amdgcn_ballot_w64(m == FILL) == 0 || spins > RETRY_CAP) break;
    }
    return true;
  };
  auto await = [&](unsigned f, unsigned a, unsigned want) {
    if (__builtin_expect(rfl(f) < want, 0))
      for (unsigned spins = 0; spins < LSPIN && rfl(lds_word(L, a)) < want; ++spins) {}
    FENCE();
  };
  auto landed = [&](v4u f, unsigned z) {
    if (__builtin_expect(rfl(min(min(f.y, f.z), f.w)) < z, 0))
      for (unsigned spins = 0; spins < LSPIN; ++spins) { const v4u t = lds_words(L, L_FLG + 16); if (rfl(min(min(t.y, t.z), t.w)) >= z) break; }
    FENCE();
  };
  auto start = [&](unsigned y, unsigned z) {
    for (unsigned spins = 0; spins < LSPIN; ++spins) {
      const v4u f = lds_words(L, L_FLG + 16);
      if (rfl(f.x) >= y && rfl(min(min(f.y, f.z), f.w)) >= z) break;
    }
    FENCE();
  };
  auto swap16 = [](unsigned& x, unsigned& y) { auto z = __builtin_amdgcn_permlane16_swap(x, y, false, false); x = z[0]; y = z[1]; };
  auto swap32 = [](unsigned& x, unsigned& y) { auto z = __builtin_amdgcn_permlane32_swap(x, y, false, false); x = z[0]; y = z[1]; };
  constexpr int PD = PDV, GW = 4, NB = NBV, TG = TGV;

#pragma unroll 1
  for (int layer = 0; layer < 2; ++layer) {
    const unsigned P = 32 * layer;
    {
      PHASE_LANE();
      constexpr int PD = 6;
      unsigned xb[4], bw[8];
      sfor<0, 4>([&](auto Q) { xb[Q.v] = act(g, layer) + XA_O + (w * 32 + ((Q.v + w) & 3) * 4) * KB; });
      sfor<0, 8>([&](auto Q) { bw[Q.v] = opaque(L_SLOT + l16 + ((Q.v >> 2) * 48 + ((Q.v + w) & 3) * 12) * KB); });
      const unsigned hb = act(g ? g - 1 : 0, layer) + XA_O + (3 * 32 + 4 * w) * KB;
      const unsigned l16h = opaque(r >= 16 ? l16 : 0x40000000u);
      float bcs[4], bps[4];
      const bool par = tok & 1;
      unsigned sel0, sel1;
      const int dq = opaque(tok - 4 * gq);
      v4u xa[32], xh[8], wq[96], fl;
      auto request = [&](auto N) {
        constexpr int i = P1T.slot[N.v];
        if constexpr (N.v == P1T.items[i]) xa[i] = ldg16(rs, l16 + (i & 3) * KB, xb[(i >> 2) & 3] + (i >> 4) * 16 * KB);
        else xh[(i >> 4) * 4 + (i & 3)] = ldg16(rs, l16h + (i & 3) * KB, hb + (i >> 4) * 16 * KB);
      };
      auto frag = [&](auto I) { constexpr int i = p1_sl(I.v); return *(LAS v4u*)(L + bw[i >> 2] + ((i & 3) * 3 + p1_mt(I.v)) * KB); };
      f32x16 aq, ak, av, hk, hv;
      f32x4 cc[2], r4[4];
      const unsigned bst = opaque(L_ST + w * 128 + r * 4), bs4 = opaque(L_ST + w * 128 + 16 * h), bc1 = opaque(L_C1 + 16 * h);
      const unsigned bex = opaque(L_EXC + w * 4096 + l16), bkv = opaque(L_KV + w * 2048 + l16), bcv = opaque(L_C1 + (64 + r) * 4);
      float rstd, mr, rsq, mrq, cv, t[48], ln[4];
      unsigned pflag, hc[8], kd[8], vd[8];
      constexpr float cs = 0.25f * 1.4426950408889634f;
      auto filler = [&](auto GI) {
        constexpr int G = GI.v;
        if constexpr (P1T.at[G] >= 0) request(IC<P1T.at[G]>{});
        if constexpr (G == 1) STAMP(20);
        if constexpr (G == 44) STAMP(21);
        if constexpr (G == 76) STAMP(22);
        if constexpr (G == 91) STAMP(23);
        constexpr int G1 = p1_gap(48 - PD);
        if constexpr (G == G1 - 5 || G == TG - 7 || G == 86) fl = lds_words(L, L_FLG + 16);
        if constexpr (G == G1 - 1) landed(fl, P + Z_1B);
        if constexpr (G == G1 + 1) mark(P + P_A);
        if constexpr (G == 90) landed(fl, P + Z_1B + 1 - layer);
        if constexpr (G == TG - 3) { await(fl.x, L_FLG + 16, P + Y_ST); rstd = *(LAS float*)(L + bst); mr = *(LAS float*)(L + bst + 640); }
        if constexpr (G == TG - 2) cv = *(LAS float*)(L + bcv);
        if constexpr (G >= TG && G < TG + 24) {
          constexpr int u = (G - TG) >> 1, lo = 4 * u + ((G - TG) & 1) * 3, hi = 4 * u + (((G - TG) & 1) ? 4 : 3);
          sfor<lo, hi>([&](auto V) { constexpr int v = V.v; t[v] = cc[u & 1][v & 3] * (v < 16 ? mr : v < 32 ? cv : mrq); PIN(t[v]); });
        }
        if constexpr (G == TG + 1) { rsq = rstd * cs; mrq = mr * cs; PIN(rsq); PIN(mrq); }
        if constexpr (G >= TG - 3 && G < TG + 21 && ((G - TG) & 1)) {
          constexpr int u = (G - TG + 3) / 2;
          if constexpr (u < 4) cc[u & 1] = *(LAS f32x4*)(L + bc1 + 128 + 32 * u);
          else if constexpr (u < 8) cc[u & 1] = *(LAS f32x4*)(L + bs4 + 640 + 32 * (u - 4));
          else cc[u & 1] = *(LAS f32x4*)(L + bc1 + 32 * (u - 8));
        }
        if constexpr (G == 72) pflag = lds_word(L, L_FLG + (w ^ 2) * 4);
        if constexpr (G == 78) { await(pflag, L_FLG + (w ^ 2) * 4, layer ? P_X2 : 0); PIN(hk); PIN(hv); }
        if constexpr (G == 79 || G == 80) *(LAS f32x4*)(L + bex + (G - 77) * 1024) = f32x4{hv[4 * G - 308], hv[4 * G - 307], hv[4 * G - 306], hv[4 * G - 305]};
        if constexpr (G >= 81 && G < 85) sfor<2 * (G - 81), 2 * (G - 81) + 2>([&](auto Q) { unsigned a = f2u(hk[Q.v]); hc[Q.v] = f2u(hk[Q.v + 8]); swap16(a, hc[Q.v]); PIN(hc[Q.v]); });
        if constexpr (G == 85) *(LAS v4u*)(L + bex) = v4u{hc[0], hc[1], hc[2], hc[3]};
        if constexpr (G == 86) *(LAS v4u*)(L + bex + 1024) = v4u{hc[4], hc[5], hc[6], hc[7]};
        if constexpr (G == 87) mark(P + P_H);
        if constexpr (G == 77 || G == 81 || G == 82 || G == 102) {
          constexpr int i = G == 77 ? 0 : G == 102 ? 3 : G - 80;
          bcs[i] = dq >= i ? 0.f : -__builtin_inff(); bps[i] = dq <= i - 2 ? 0.f : -__builtin_inff(); PIN(bcs[i]); PIN(bps[i]);
        }
        if constexpr (G == 86) { sel0 = par ? 0x01000c0cu : 0x0c0c0100u; sel1 = sel0 + (par ? 0x02020000u : 0x00000202u); PIN(sel0); PIN(sel1); }
        if constexpr (G >= 88 && G < 92) r4[G - 88] = *(LAS f32x4*)(L + bs4 + 32 * (G - 88));
        if constexpr (G >= 92 && G < 100) {
          constexpr int i = G - 92;
          ln[0] = fmaf(ak[2 * i], rstd, t[2 * i]); PIN(ln[0]); ln[1] = fmaf(ak[2 * i + 1], rstd, t[2 * i + 1]); PIN(ln[1]);
          if constexpr (i > 0) { kd[i - 1] = pk(ln[2], ln[3]); PIN(kd[i - 1]); }
          ln[2] = ln[0]; ln[3] = ln[1];
        }
        if constexpr (G == 100) { kd[7] = pk(ln[2], ln[3]); swap16(kd[0], kd[2]); swap16(kd[1], kd[3]); PIN(kd[0]); PIN(kd[1]); PIN(kd[2]); PIN(kd[3]); PIN(kd[7]); }
        if constexpr (G == 101) { swap16(kd[4], kd[6]); swap16(kd[5], kd[7]); PIN(kd[4]); PIN(kd[5]); PIN(kd[6]); PIN(kd[7]); }
        if constexpr (G >= 103 && G < 111) {
          constexpr int i = (G - 103) ^ 4, ip = (G - 104) ^ 4;
          ln[0] = fmaf(av[2 * i], r4[i >> 1][2 * (i & 1)], t[16 + 2 * i]); PIN(ln[0]); ln[1] = fmaf(av[2 * i + 1], r4[i >> 1][2 * (i & 1) + 1], t[16 + 2 * i + 1]); PIN(ln[1]);
          if constexpr (G > 103) { vd[ip] = pk(ln[2], ln[3]); PIN(vd[ip]); }
          ln[2] = ln[0]; ln[3] = ln[1];
          if constexpr (G == 108) { swap32(vd[4], vd[6]); swap32(vd[5], vd[7]); PIN(vd[4]); PIN(vd[5]); PIN(vd[6]); PIN(vd[7]); }
          if constexpr (G == 109) { swap16(vd[4], vd[6]); swap16(vd[5], vd[7]); PIN(vd[4]); PIN(vd[5]); PIN(vd[6]); PIN(vd[7]); }
          if constexpr (G == 110) *(LAS v4u*)(L + bkv + 2048) = v4u{kd[2], kd[3], vd[4], vd[5]};
        }
        if constexpr (G == 111) {
          *(LAS v4u*)(L + bkv + 3072) = v4u{kd[6], kd[7], vd[6], vd[7]};
          mark(P + P_K);
          vd[3] = pk(ln[2], ln[3]); PIN(vd[3]);
        }
      };
      static_assert(p1_gap(60) == 76 && p1_gap(74) == 90 && p1_gap(85) == 101 && p1_gap(96) == 112, "");
      SB();
      for (unsigned spins = 0; spins < LSPIN && rfl(lds_word(L, L_FLG + 16)) < P + Y_X; ++spins) {}
      FENCE();
      STAMP(1);
      sfor<0, NB - 2>([&](auto N) { request(N); SB(); });
      landed(lds_words(L, L_FLG + 16), P + Z_1A);
      sfor<0, PD>([&](auto I) { wq[I.v] = frag(I); });
      SB();
      sfor<NB - 2, NB>([&](auto N) { request(N); SB(); });
      sfor<0, 96>([&](auto I) {
        constexpr int s = I.v, i = p1_sl(s), mt = p1_mt(s);
        if constexpr (s % 3 == 0 && s + PD <= 96) WAITL(PD - 3);
        if constexpr (mt == 0) mma32<i == 0>(aq, wq[s], xa[i]);
        if constexpr (mt == 1) mma32<i == 0>(ak, wq[s], xa[i]);
        if constexpr (mt == 2) mma32<i == 0>(av, xa[i], wq[s]);
        SB();
        if constexpr (s + PD < 96) wq[s + PD] = frag(IC<s + PD>{});
        filler(IC<p1_gap(s)>{});
        SB();
        if constexpr (p1_two(s)) {
          if constexpr (mt == 1) mma32<i == 0>(hk, wq[s], xh[(i >> 4) * 4 + (i & 3)]); else mma32<i == 0>(hv, xh[(i >> 4) * 4 + (i & 3)], wq[s]);
          SB();
          filler(IC<p1_gap(s) + 1>{});
          SB();
        }
      });
      const unsigned pa = w ? L_FLG + (w - 1) * 4 : L_FLG + 16, pw = P + (w ? P_K : Y_KV);
      pflag = lds_word(L, pa);
      swap32(vd[0], vd[2]); swap32(vd[1], vd[3]);
      SB();
      asm volatile("s_nop 6");
      swap16(vd[0], vd[2]); swap16(vd[1], vd[3]);
      SB();
      STAMP(2);
      unsigned qd[8];
      {
        float y[16];
        sfor<0, 16>([&](auto I) { y[I.v] = fmaf(aq[I.v], rsq, t[32 + I.v]); PIN(y[I.v]); });
        SB();
        await(pflag, pa, pw);
        SB();
        sfor<0, 8>([&](auto I) { qd[I.v] = pk(y[2 * I.v], y[2 * I.v + 1]); });
      }
      v2u kp[2], vp[2];
      sfor<0, 2>([&](auto E) { kp[E.v] = *(LAS v2u*)(L + bkv + E.v * 1024); vp[E.v] = *(LAS v2u*)(L + bkv + E.v * 1024 + 8); });
      SB();
      swap16(qd[0], qd[2]); swap16(qd[1], qd[3]); swap16(qd[4], qd[6]); swap16(qd[5], qd[7]);
      STAMP(3);
      f32x4 sc[4], sp[4], bp = {bps[0], bps[1], bps[2], bps[3]};
      const f32x4 bc = {bcs[0], bcs[1], bcs[2], bcs[3]};
      sfor<0, 2>([&](auto E) {
        constexpr int e = E.v;
        sc[2 + e] = mfma16k16(v2u{kd[4 * e + 2], kd[4 * e + 3]}, v2u{qd[4 * e + 2], qd[4 * e + 3]}, bc);
        sp[2 + e] = mfma16k16(v2u{kd[4 * e], kd[4 * e + 1]}, v2u{qd[4 * e + 2], qd[4 * e + 3]}, bp);
      });
      sfor<0, 2>([&](auto E) { constexpr int e = E.v; sc[e] = mfma16k16(v2u{kd[4 * e], kd[4 * e + 1]}, v2u{qd[4 * e], qd[4 * e + 1]}, bc); });
      SB();
      if (t32a == 0) { bp = f32x4{-__builtin_inff(), -__builtin_inff(), -__builtin_inff(), -__builtin_inff()}; PIN(bp); }
      sfor<0, 2>([&](auto E) { constexpr int e = E.v; sp[e] = mfma16k16(kp[e], v2u{qd[4 * e], qd[4 * e + 1]}, bp); });
      SB();
      if constexpr (A_O == 3) mark(P + P_O);
      float a[4][4], mx[4];
      sfor<0, 4>([&](auto C) {
        constexpr int c = C.v ^ 2;
        a[c][2] = pick(par, sc[c][0], sc[c][1]); a[c][3] = pick(par, sc[c][2], sc[c][3]);
        a[c][0] = pick(par, sp[c][0], sp[c][1]); a[c][1] = pick(par, sp[c][2], sp[c][3]);
        SB();
      });
      sfor<0, 4>([&](auto C) { mx[C.v ^ 2] = vmax3(a[C.v ^ 2][0], a[C.v ^ 2][1], a[C.v ^ 2][2]); });
      SB();
      sfor<0, 4>([&](auto C) { mx[C.v ^ 2] = vmax(mx[C.v ^ 2], a[C.v ^ 2][3]); });
      SB();
      {
        unsigned x0 = f2u(mx[0]), x1 = f2u(mx[1]), x2 = f2u(mx[2]), x3 = f2u(mx[3]);
        asm volatile("s_nop 1" : "+v"(x0), "+v"(x1), "+v"(x2), "+v"(x3));
        swap16(x2, x3); swap16(x0, x1);
        unsigned y0 = f2u(vmax(u2f(x0), u2f(x1))), y1 = f2u(vmax(u2f(x2), u2f(x3)));
        asm volatile("s_nop 1" : "+v"(y0), "+v"(y1));
        swap32(y0, y1);
        unsigned z0 = f2u(vmax(u2f(y0), u2f(y1))), z1 = z0;
        swap32(z0, z1);
        unsigned z2 = z0, z3 = z1;
        swap16(z0, z2); swap16(z1, z3);
        mx[0] = u2f(z0); mx[1] = u2f(z2); mx[2] = u2f(z1); mx[3] = u2f(z3);
      }
      SB();
      sfor<0, 16>([&](auto I) { a[(I.v >> 2) ^ 2][I.v & 3] -= mx[(I.v >> 2) ^ 2]; PIN(a[(I.v >> 2) ^ 2][I.v & 3]); });
      SB();
      if constexpr (A_O == 2) mark(P + P_O);
      const f32x4 z4 = {0, 0, 0, 0};
      const v4u ones = {0x3f803f80u, 0x3f803f80u, 0x3f803f80u, 0x3f803f80u};
      f32x4 o[4], l[4];
      sfor<0, 2>([&](auto H) {
        constexpr int c0 = 2 - 2 * H.v;
        sfor<0, 8>([&](auto I) { a[c0 + (I.v >> 2)][I.v & 3] = __builtin_amdgcn_exp2f(a[c0 + (I.v >> 2)][I.v & 3]); });
        SB();
        unsigned pc[4];
        sfor<0, 4>([&](auto I) { pc[I.v] = pk(a[c0 + (I.v >> 1)][2 * (I.v & 1)], a[c0 + (I.v >> 1)][2 * (I.v & 1) + 1]); });
        SB();
        v4u pf[2];
        sfor<0, 2>([&](auto E) {
          pf[E.v] = v4u{__builtin_amdgcn_perm(0u, pc[2 * E.v], sel0), __builtin_amdgcn_perm(0u, pc[2 * E.v], sel1), __builtin_amdgcn_perm(0u, pc[2 * E.v + 1], sel0), __builtin_amdgcn_perm(0u, pc[2 * E.v + 1], sel1)};
        });
        SB();
        sfor<0, 2>([&](auto E) {
          constexpr int e = E.v;
          l[c0 + e] = mfma16(ones, pf[e], z4);
          o[c0 + e] = mfma16(c0 ? v4u{vd[2 * e], vd[2 * e + 1], vd[4 + 2 * e], vd[5 + 2 * e]} : v4u{vp[e].x, vp[e].y, vd[2 * e], vd[2 * e + 1]}, pf[e], z4);
        });
        SB();
      });
      if constexpr (A_O == 1) mark(P + P_O);
      const unsigned ov = ((gq & 1) * 64 + tok + 32 * (gq >> 1)) * 16, os = act(g, 0) + O32_O + (w * 32 + 2 * j) * KB;
      sfor<0, 2>([&](auto H) {
        constexpr int c0 = 2 - 2 * H.v;
        float il[2], y[8];
        sfor<0, 2>([&](auto E) { il[E.v] = __builtin_amdgcn_rcpf(l[c0 + E.v][0]); });
        sfor<0, 8>([&](auto I) { y[I.v] = o[c0 + (I.v >> 2)][I.v & 3] * il[I.v >> 2]; PIN(y[I.v]); });
        SB();
        unsigned d[4];
        sfor<0, 4>([&](auto I) { d[I.v] = nf(pk(y[2 * I.v], y[2 * I.v + 1])); });
        swap16(d[0], d[2]); swap16(d[1], d[3]);
        stg16(v4u{d[0], d[1], d[2], d[3]}, rs, ov + c0 * 128, os);
        SB();
      });
      if constexpr (A_O == 0) mark(P + P_O);
      STAMP(4);
    }
    auto finish = [&](f32x16 (&acc)[2], unsigned l16, int w, unsigned pe) {
      const unsigned be = opaque(L_EXC + w * 4096 + l16), bp = opaque(L_EXC + (w ^ 2) * 4096 + l16);
#pragma unroll
      for (int q = 0; q < 4; ++q) *(LAS f32x4*)(L + be + q * 1024) = f32x4{acc[1][4 * q], acc[1][4 * q + 1], acc[1][4 * q + 2], acc[1][4 * q + 3]};
      mark(pe);
      for (unsigned spins = 0; spins < LSPIN && rfl(lds_word(L, L_FLG + (w ^ 2) * 4)) < pe; ++spins) {}
      FENCE();
      f32x4 p[4];
#pragma unroll
      for (int q = 0; q < 4; ++q) p[q] = *(LAS f32x4*)(L + bp + q * 1024);
      SB();
#pragma unroll
      for (int q = 0; q < 4; ++q) {
        xr[2 * q] = nf(pk(acc[0][4 * q] + p[q][0], acc[0][4 * q + 1] + p[q][1]));
        xr[2 * q + 1] = nf(pk(acc[0][4 * q + 2] + p[q][2], acc[0][4 * q + 3] + p[q][3]));
      }
    };
    {
      PHASE_LANE();
      constexpr int PD = 6;
      f32x16 acc[2], seed;
#pragma unroll
      for (int i = 0; i < 8; ++i) { seed[2 * i] = blo(xr[i]); seed[2 * i + 1] = bhi(xr[i]); }
      PIN(seed);
      const unsigned ob = act(g, 0) + O32_O + (tb * 32 + 16 * kh) * KB;
      unsigned bw[4];
      sfor<0, 4>([&](auto Q) { bw[Q.v] = opaque(L_SLOT + ((Q.v >> 1) ? 64 + 48 * kh : 48 + 32 * kh) * KB + tr_lane2(lane, false, kh ^ (Q.v & 1))); });
      v4u of[16], wq[32];
      auto frag = [&](auto I) { return read_tr2(L + bw[(I.v >> 4) * 2 + (I.v & 1)] + ((I.v >> 1) & 7) * 2 * KB, 512); };
      auto request = [&](auto N) { of[N.v] = ldg16(rs, l16 + (N.v & 3) * KB, ob + (N.v >> 2) * 4 * KB); };
      unsigned second = 0;
      do {
        SB();
        start(P + Y_O, P + Z_2);
        STAMP(5);
        request(IC<0>{});
        sfor<0, PD>([&](auto I) { wq[I.v] = frag(I); });
        SB();
        sfor<1, NB>([&](auto N) { request(N); SB(); });
        wstream<0, 32, 32, PD, 3, 2>(wq, frag,
          [&](auto I, v4u wf) { if constexpr (I.v == 0) mma32c(acc[0], wf, of[0], seed); else mma32<I.v == 1>(acc[I.v & 1], wf, of[I.v >> 1]); },
          [&](auto I) {
            constexpr int n = (I.v >> 1) + NB;
            if constexpr (!(I.v & 1) && n < 16) request(IC<n>{});
            if constexpr (I.v == A_X1) mark(P + P_X1);
            if constexpr (I.v == 0) STAMP(24);
          });
        MFMA_DONE();
        SB();
      } while (again(acc[1][0], ob, 16, second));
      STAMP(6);
      finish(acc, l16, w, P + P_E2);
      publish(act(g, 0) + X32_O + (tb * 32 + 2 * cbk) * KB, act(g, 0) + STB_O + (tb * 16 + cbk) * 256, l16, r, h);
#pragma unroll
      for (int c = 0; c < 2; ++c) *(LAS v4u*)(L + L_KV + (w + 1) * 2048 + c * 1024 + l16) = v4u{xr[4 * c], xr[4 * c + 1], xr[4 * c + 2], xr[4 * c + 3]};
      STAMP(7);
    }
    {
      PHASE_LANE();
      const unsigned xb = act(g, 0) + X32_O + w * 32 * KB;
      const unsigned hb = act(g, 0) + H32_O + w * 86 * KB;
      const unsigned lo = opaque(L_SLOT + l16), hi = opaque(L_SLOT + 65536 + l16);
      const unsigned bc3 = opaque(L_C3 + 16 * h), bsv = opaque(L_ST + (w * 32 + r) * 16);
      f32x4 cg, cu;
      v4u xa[32], wq[192], fl;
      float nmean, k1, k2, vv;
      float ea[4], eb[4], et[4];
      unsigned eo[4];
      f32x16 ac[3];
      auto ep = [&](auto K) {
        constexpr int lu = K.v / EP_N, k = K.v % EP_N, q = k / 43, o = ep_op(k), e = o & 3, sg = o >> 2, i0 = 4 * q + e;
        if constexpr (o == 42) {
          constexpr int off = q == 0 ? lu * 128 + 32 : (lu + 1) * 128;
          cg = *(LAS f32x4*)(L + bc3 + off); cu = *(LAS f32x4*)(L + bc3 + off + 64);
        } else if constexpr (o == 43) stg16(v4u{eo[0], eo[1], eo[2], eo[3]}, rs, l16, hb + hpos(j, lu) * KB);
        else if constexpr (sg == 0) { ea[e] = fmaf(cg[e], nmean, ac[lu % 3][i0]); PIN(ea[e]); }
        else if constexpr (sg == 1) { eb[e] = fmaf(cu[e], nmean, ac[lu % 3][8 + i0]); PIN(eb[e]); }
        else if constexpr (sg == 2) { et[e] = ea[e] * ea[e]; PIN(et[e]); }
        else if constexpr (sg == 3) { et[e] = fmaf(et[e], k1, k2); PIN(et[e]); }
        else if constexpr (sg == 4) { et[e] *= ea[e]; PIN(et[e]); }
        else if constexpr (sg == 5) { eb[e] *= ea[e]; PIN(eb[e]); }
        else if constexpr (sg == 6) { et[e] = __builtin_amdgcn_exp2f(et[e]); PIN(et[e]); }
        else if constexpr (sg == 7) { et[e] = fmaf(et[e], vv, vv); PIN(et[e]); }
        else if constexpr (sg == 8) { et[e] = __builtin_amdgcn_rcpf(et[e]); PIN(et[e]); }
        else if constexpr (sg == 9) { eb[e] *= et[e]; PIN(eb[e]); }
        else { eo[2 * q + e] = pk(eb[2 * e], eb[2 * e + 1]); PIN(eo[2 * q + e]); }
        SB();
      };
      auto at = [&](int kb) { return kb < 64 ? L + lo + kb * KB : L + hi + (kb - 64) * KB; };
      auto frag = [&](auto I) { return *(LAS v4u*)at((I.v >> 6) == 1 ? mapY(I.v & 63) : mapX(I.v & 63)); };
      auto request = [&](auto N) { xa[N.v] = ldg16(rs, l16 + (N.v & 3) * KB, xb + (N.v >> 2) * 4 * KB); };
      auto p3_mma = [&](auto I, v4u wf) {
        constexpr int i = I.v;
        if constexpr (i < 64) mma32<(i < 2)>(ac[i & 1], wf, xa[i >> 1]);
        else mma32<(i & 31) == 0>(ac[(i >> 5) % 3], wf, xa[i & 31]);
      };
      auto p3_extra = [&](auto I) {
        constexpr int i = I.v;
        if constexpr (!(i & 1) && (i >> 1) + NB < 32) request(IC<(i >> 1) + NB>{});
        if constexpr (i == 59 - PD || i == 59 || i == 123 - PD || i == 186) fl = lds_words(L, L_FLG + 16);
        if constexpr (i == 63 - PD) landed(fl, P + Z_31);
        if constexpr (i == 63) { await(fl.x, L_FLG + 16, P + Y_S3); const f32x4 sv = *(LAS f32x4*)(L + bsv); nmean = sv[0]; k1 = sv[1]; k2 = sv[2]; vv = sv[3]; }
        if constexpr (i == 61) { cg = *(LAS f32x4*)(L + bc3); cu = *(LAS f32x4*)(L + bc3 + 64); }
        if constexpr (i == 127 - PD) landed(fl, P + Z_32);
        if constexpr (i == 128 - PD) mark(P + P_31);
        if constexpr (i == 138) { static_assert(ep_done(119) >= 2 * EP_N && ep_done(137) < 3 * EP_N, ""); WAITV(0); mark(P + P_H0); }
        if constexpr (i == 160 - PD) mark(P + P_U4);
        if constexpr (i == 192 - PD) mark(P + P_32);
        if constexpr (i == 188) { static_assert(ep_done(168) >= 4 * EP_N && ep_done(187) < 5 * EP_N, ""); WAITV(0); mark(P + P_H1); }
        if constexpr (i >= 67) sfor<ep_done(i - 1), ep_done(i)>(ep);
        if constexpr (i == 0) STAMP(25);
        if constexpr (i == 63) STAMP(9);
        if constexpr (i == 127) STAMP(10);
      };
      static_assert(ep_done(95) >= 52 && ep_done(127) >= EP_N + 52 && ep_done(159) >= 2 * EP_N + 52, "an accumulator is read out before it is reused");
      static_assert(ep_done(98) <= 2 * EP_N && ep_done(130) <= 3 * EP_N && ep_done(162) <= 4 * EP_N, "an accumulator is complete before it is read");
      unsigned second = 0;
      do {
        SB();
        start(P + Y_X1, P + Z_30);
        STAMP(8);
        request(IC<0>{});
        sfor<0, PD>([&](auto I) { wq[I.v] = frag(I); });
        SB();
        sfor<1, NB>([&](auto N) { request(N); SB(); });
        wstream<0, 66, 192, PD, GW, 1>(wq, frag, p3_mma, p3_extra);
      } while (again(ac[0][0], xb, 32, second));
      mark(P + P_30);
      wstream<66, 192, 192, PD, GW, 1>(wq, frag, p3_mma, p3_extra);
      STAMP(11);
      const unsigned hb4 = act(g, 0) + H32_O + (tb * 86 + 43 * kh) * KB;
      v4u hf[43];
      auto ldh = [&](auto N) { hf[N.v] = ldg16(rs, l16 + (N.v & 3) * KB, hb4 + (N.v >> 2) * 4 * KB); };
      await(fl.x, L_FLG + 16, P + Y_H0);
      STAMP(12);
      const f32x4 cg2 = *(LAS f32x4*)(L + bc3 + 5 * 128 + 32), cu2 = *(LAS f32x4*)(L + bc3 + 5 * 128 + 96);
      const v4u xp0 = *(LAS v4u*)(L + L_KV + (w + 1) * 2048 + l16), xp1 = *(LAS v4u*)(L + L_KV + (w + 1) * 2048 + 1024 + l16);
      fl = lds_words(L, L_FLG + 16);
      sfor<0, 5>([&](auto I) { ldh(I); SB(); });
      MFMA_DONE();
      SB();
      {
        float ya[8], yb[8], yt[8];
        sfor<0, 8>([&](auto I) { ya[I.v] = fmaf(I.v < 4 ? cg[I.v & 3] : cg2[I.v & 3], nmean, ac[2][I.v]); yb[I.v] = fmaf(I.v < 4 ? cu[I.v & 3] : cu2[I.v & 3], nmean, ac[2][8 + I.v]); });
        SB();
        sfor<0, 8>([&](auto I) { yt[I.v] = fmaf(ya[I.v] * ya[I.v], k1, k2) * ya[I.v]; });
        SB();
        sfor<0, 8>([&](auto I) { yt[I.v] = __builtin_amdgcn_exp2f(yt[I.v]); yb[I.v] *= ya[I.v]; });
        SB();
        sfor<0, 8>([&](auto I) { yt[I.v] = __builtin_amdgcn_rcpf(fmaf(yt[I.v], vv, vv)); });
        SB();
        sfor<0, 8>([&](auto I) { yb[I.v] *= yt[I.v]; });
        stg16(v4u{pk(yb[0], yb[1]), pk(yb[2], yb[3]), pk(yb[4], yb[5]), pk(yb[6], yb[7])}, rs, six ? l16 : 0x40000000u, hb + hpos(j, 5) * KB);
      }
      SB();
      STAMP(13);
      constexpr int PD4 = 6;
      const unsigned o0 = L_SLOT + kh * 2 * KB + tr_lane2(lane, true, kh);
      const unsigned b4[4] = {(unsigned)opaque(o0), (unsigned)opaque(o0 ^ 64), (unsigned)opaque(o0 + 65536), (unsigned)opaque((o0 ^ 64) + 65536)};
      v4u w4q[86];
      auto frag4 = [&](auto I) {
        constexpr int f = 4 * ((I.v >> 1) & 15), kb = I.v < 32 ? mapY(f) : I.v < 64 ? mapX(f) : mapZ(f);
        return read_tr2(L + b4[(kb >> 6) * 2 + (I.v & 1)] + (kb & 63) * KB, 1024);
      };
      ldh(IC<5>{});
      landed(fl, P + Z_40);
      sfor<0, PD4>([&](auto I) { w4q[I.v] = frag4(I); });
      SB();
      f32x16 acc[2];
      sfor<0, 4>([&](auto I) { acc[0][2 * I.v] = blo(xp0[I.v]); acc[0][2 * I.v + 1] = bhi(xp0[I.v]); acc[0][8 + 2 * I.v] = blo(xp1[I.v]); acc[0][9 + 2 * I.v] = bhi(xp1[I.v]); });
      PIN(acc[0]);
      sfor<6, NB>([&](auto I) { ldh(I); SB(); });
      wstream<0, 86, 86, PD4, 3, 2>(w4q, frag4,
        [&](auto I, v4u wf) { mma32<I.v == 1>(acc[I.v & 1], wf, hf[I.v >> 1]); },
        [&](auto I) {
          constexpr int i = I.v, n = (i >> 1) + NB;
          constexpr int g1 = 2 * (16 - NB), g2 = 2 * (32 - NB);
          if constexpr (i == g1 - 4 || i == 27 - PD4 || i == g2 - 4 || i == 59 - PD4) fl = lds_words(L, L_FLG + 16);
          if constexpr (i == 13) { static_assert(g1 > 13, ""); asm volatile("s_waitcnt vmcnt(%0)" :: "i"(NB - 5 + 7) : "memory"); mark(P + P_H2); }
          if constexpr (i == g1) await(fl.x, L_FLG + 16, P + Y_H1);
          if constexpr (i == g2) await(fl.x, L_FLG + 16, P + Y_H2);
          if constexpr (!(i & 1) && n < 43) ldh(IC<n>{});
          if constexpr (i == 31 - PD4) landed(fl, P + Z_41);
          if constexpr (i == 32 - PD4) mark(P + P_40);
          if constexpr (i == 63 - PD4) landed(fl, P + Z_42);
          if constexpr (i == 64 - PD4) mark(P + P_41);
          if constexpr (i == 0) STAMP(14);
          if constexpr (i == g1) STAMP(15);
          if constexpr (i == g2) STAMP(16);
        });
      MFMA_DONE();
      SB();
      STAMP(17);
      finish(acc, l16, w, P + P_E4);
      if (layer == 0) {
        publish(act(g, 1) + XA_O + (tb * 32 + 2 * cbk) * KB, act(g, 1) + STA_O + (tb * 16 + cbk) * 256, l16, r, h);
        STAMP(18);
        DRAIN();
        mark(P_X2);
      } else {
#pragma unroll
        for (int q = 0; q < 4; ++q) *(v2u*)(out + (size_t)(32 * t32b + r) * D_ + 32 * cbk + 8 * q + 4 * h) = v2u{xr[2 * q], xr[2 * q + 1]};
        STAMP(18);
      }
#pragma unroll
      for (int i = 0; i < 8; ++i) PIN(xr[i]);
    }
  }
  STAMP_OUT();
}
}

extern "C" void kernel_launch(void* const* d_in, const int* in_sizes, int n_in, void* d_out, int out_size, void* d_ws, size_t ws_size, hipStream_t stream) {
  const unsigned short* x = (const unsigned short*)d_in[0];
  const unsigned short* wqkv = (const unsigned short*)d_in[1];
  const unsigned short* wo = (const unsigned short*)d_in[2];
  const unsigned short* wg = (const unsigned short*)d_in[4];
  const unsigned short* wu = (const unsigned short*)d_in[5];
  const unsigned short* wd = (const unsigned short*)d_in[6];
  unsigned char* ws = (unsigned char*)d_ws;
  (void)hipFuncSetAttribute((const void*)k_main, hipFuncAttributeMaxDynamicSharedMemorySize, L_END);
  k_main<<<NWG, 512, L_END, stream>>>(x, wqkv, wo, wg, wu, wd, (unsigned short*)d_out, ws);
}
```
